```python
import torch

def _softmax(x, axis=-1):
    return torch.softmax(x, dim=axis)

def _gelu(x):
    return torch.nn.functional.gelu(x, approximate='tanh')

def _silu(x):
    return torch.nn.functional.silu(x)

def _sigmoid(x):
    return torch.sigmoid(x)

def _layernorm(x, axis=-1, eps=1e-05):
    m = x.mean(dim=axis, keepdim=True)
    v = x.var(dim=axis, keepdim=True, unbiased=False)
    return (x - m) / torch.sqrt(v + eps)

def _scatter_add(tgt, idx, src, axis=0):
    return tgt.clone().index_add_(axis, idx, src)

def _segment_sum(src, seg, n):
    out = torch.zeros((n,) + src.shape[1:], dtype=src.dtype)
    return out.index_add_(0, seg, src)

def _pad(x, pw):
    flat = tuple((v for pair in reversed(pw) for v in pair))
    return torch.nn.functional.pad(x, flat)
kernel_name = 'adaLN_cond__gqa_block_cached__swiglu_ffn__decode'

def setup_inputs():

    _w = lambda a: (lambda t: t.to(torch.bfloat16) if t.is_floating_point() else t)(torch.as_tensor(a))

    def _A_setup():
        B, Sq, D = (1, 1, 640)
        Hq, Hkv, Sk, Dh = (32, 32, 8192, 128)
        assert Hq % Hkv == 0, (Hq, Hkv)
        return {'x': _w((lambda *s: torch.randn(*s, dtype=torch.bfloat16) * 0.05)(B, Sq, D)), 'k_cache': _w((lambda *s: torch.randn(*s, dtype=torch.bfloat16) * 3.2)(B, Hkv, Sk, Dh)), 'v_cache': _w((lambda *s: torch.randn(*s, dtype=torch.bfloat16) * 0.4)(B, Hkv, Sk, Dh)), 'wq': _w((lambda *s: torch.randn(*s, dtype=torch.bfloat16) * 3.2)(D, Hq * Dh) * D ** (-0.5)), 'wo': _w((lambda *s: torch.randn(*s, dtype=torch.bfloat16) * 0.4)(Hq * Dh, D) * (Hq * Dh) ** (-0.5))}
    _A_in = _A_setup()

    def _B_setup():
        B, S, D, F = (1, 1, 640, 1728)
        return {'x': _w((lambda *s: torch.randn(*s, dtype=torch.bfloat16) * 0.05)(B, S, D)), 'w_gate': _w((lambda *s: torch.randn(*s, dtype=torch.bfloat16) * 0.05)(D, F)), 'w_up': _w((lambda *s: torch.randn(*s, dtype=torch.bfloat16) * 0.05)(D, F)), 'w_down': _w((lambda *s: torch.randn(*s, dtype=torch.bfloat16) * 0.05)(F, D))}
    _B_in = _B_setup()
    return {'A_x': _A_in['x'], 'A_k_cache': _A_in['k_cache'], 'A_v_cache': _A_in['v_cache'], 'A_wq': _A_in['wq'], 'A_wo': _A_in['wo'], 'B_x': _B_in['x'], 'B_w_gate': _B_in['w_gate'], 'B_w_up': _B_in['w_up'], 'B_w_down': _B_in['w_down'], 'c': _w((lambda *s: torch.randn(*s, dtype=torch.bfloat16) * 0.1)(1, 640) * 8.0), 'w_mod': _w((lambda *s: torch.randn(*s, dtype=torch.bfloat16) * 0.1)(640, 6 * 640) * 640 ** (-0.5) * 8.0)}

def reference(A_x, A_k_cache, A_v_cache, A_wq, A_wo, B_x, B_w_gate, B_w_up, B_w_down, c, w_mod):

    def _A_ref(x, k_cache, v_cache, wq, wo):
        B, Sq, D = x.shape
        Hq, Dh = (32, 128)
        Hkv = k_cache.shape[1]
        rep = Hq // Hkv
        q = (lambda x, *d: x.permute(*d))((x @ wq).reshape(B, Sq, Hq, Dh), 0, 2, 1, 3)
        k = (lambda x, n, axis: torch.repeat_interleave(x, n, dim=axis))(k_cache, rep, axis=1)
        v = (lambda x, n, axis: torch.repeat_interleave(x, n, dim=axis))(v_cache, rep, axis=1)
        scores = torch.einsum('bhqd,bhkd->bhqk', q, k) * Dh ** (-0.5)
        p = _softmax(scores, axis=-1)
        o = torch.einsum('bhqk,bhkd->bhqd', p, v)
        return (lambda x, *d: x.permute(*d))(o, 0, 2, 1, 3).reshape(B, Sq, Hq * Dh) @ wo

    def _B_ref(x, w_gate, w_up, w_down):
        g = _silu(x @ w_gate)
        u = x @ w_up
        return g * u @ w_down
    x = A_x
    mod = (_silu(c) @ w_mod).reshape(c.shape[0], 6, 1, -1)
    sa, ca, ga, sb, cb, gb = (mod[:, 0].clone(), mod[:, 1].clone(), mod[:, 2].clone(), mod[:, 3].clone(), mod[:, 4].clone(), mod[:, 5].clone())
    h = _layernorm(x, axis=-1) * (1 + ca) + sa
    x = x + ga * _A_ref(h, A_k_cache, A_v_cache, A_wq, A_wo)
    h = _layernorm(x, axis=-1) * (1 + cb) + sb
    x = x + gb * _B_ref(h, B_w_gate, B_w_up, B_w_down)
    return x
```

```cpp
#include <hip/hip_runtime.h>
#include <hip/hip_bf16.h>
#include <stdint.h>

#define MIMIC 0

typedef unsigned short u16;
constexpr int D = 640, HQ = 32, SK = 8192, DH = 128, F = 1728;

__device__ __forceinline__ float bf2f(u16 b) { return __uint_as_float(((unsigned)b) << 16); }
__device__ __forceinline__ float rbf(float f) {
  return f;
}
__device__ __forceinline__ u16 f2bf(float f) { unsigned u = __float_as_uint(f); u += 0x7FFFu + ((u >> 16) & 1u); return (u16)(u >> 16); }

__device__ float block_sum(float v, float* red) {
  for (int m = 32; m >= 1; m >>= 1) v += __shfl_xor(v, m);
  int w = threadIdx.x >> 6, nw = blockDim.x >> 6;
  __syncthreads();
  if ((threadIdx.x & 63) == 0) red[w] = v;
  __syncthreads();
  float s = 0; for (int i = 0; i < nw; ++i) s += red[i];
  return s;
}

__global__ void k_mod(const u16* c, const u16* wmod, float* mod) {
  __shared__ float sc[D];
  for (int i = threadIdx.x; i < D; i += blockDim.x) { float v = bf2f(c[i]); sc[i] = rbf(v / (1.f + __expf(-v))); }
  __syncthreads();
  int n = blockIdx.x * blockDim.x + threadIdx.x;
  float acc = 0;
  for (int k = 0; k < D; ++k) acc += sc[k] * bf2f(wmod[(size_t)k * 6 * D + n]);
  mod[n] = rbf(acc);
}

__device__ void ln_mod(const float* x, const float* cc, const float* ss, float* h, float* red) {
  float s = 0; for (int i = threadIdx.x; i < D; i += blockDim.x) s += x[i];
  float mean = block_sum(s, red) / D;
  float v = 0; for (int i = threadIdx.x; i < D; i += blockDim.x) { float d = x[i] - mean; v += d * d; }
  float var = block_sum(v, red) / D;
  float mb = rbf(mean), vb = rbf(var);
  float den = rbf(sqrtf(rbf(vb + 1e-5f)));
  for (int i = threadIdx.x; i < D; i += blockDim.x) {
    float t = rbf(rbf(x[i] - mb) / den);
    h[i] = rbf(rbf(t * rbf(1.f + cc[i])) + ss[i]);
  }
  __syncthreads();
}

__global__ void k_q(const u16* x, const float* mod, const u16* wq, float* q) {
  __shared__ float sx[D], sh[D], red[16];
  for (int i = threadIdx.x; i < D; i += blockDim.x) sx[i] = bf2f(x[i]);
  __syncthreads();
  ln_mod(sx, mod + D, mod, sh, red);
  int n = blockIdx.x * blockDim.x + threadIdx.x;
  float acc = 0;
  for (int k = 0; k < D; ++k) acc += sh[k] * bf2f(wq[(size_t)k * HQ * DH + n]);
  q[n] = rbf(acc);
}

__global__ void k_scores(const float* q, const u16* K, float* sc) {
  __shared__ float sq[DH];
  int h = blockIdx.y;
  if (threadIdx.x < DH) sq[threadIdx.x] = q[h * DH + threadIdx.x];
  __syncthreads();
  int key = blockIdx.x * 256 + threadIdx.x;
  const u16* kr = K + ((size_t)h * SK + key) * DH;
  float acc = 0;
  for (int d = 0; d < DH; ++d) acc += sq[d] * bf2f(kr[d]);
  sc[h * SK + key] = rbf(rbf(acc) * 0.08838834764831845f);
}

__global__ void k_pv(const float* sc, const u16* V, float* o) {
  __shared__ float p[SK]; __shared__ float red[16]; __shared__ float part[8][DH];
  int h = blockIdx.x;
  float m = -INFINITY;
  for (int i = threadIdx.x; i < SK; i += blockDim.x) m = fmaxf(m, sc[h * SK + i]);
  for (int k = 32; k >= 1; k >>= 1) m = fmaxf(m, __shfl_xor(m, k));
  if ((threadIdx.x & 63) == 0) red[threadIdx.x >> 6] = m;
  __syncthreads();
  for (int i = 0; i < 16; ++i) m = fmaxf(m, red[i]);
  float s = 0;
  for (int i = threadIdx.x; i < SK; i += blockDim.x) { float e = __expf(sc[h * SK + i] - m); p[i] = e; s += e; }
  float L = block_sum(s, red);
  for (int i = threadIdx.x; i < SK; i += blockDim.x) p[i] = rbf(p[i] / L);
  __syncthreads();
  int d = threadIdx.x & 127, g = threadIdx.x >> 7;
  float acc = 0;
  for (int k = g; k < SK; k += 8) acc += p[k] * bf2f(V[((size_t)h * SK + k) * DH + d]);
  part[g][d] = acc;
  __syncthreads();
  if (threadIdx.x < DH) { float t = 0; for (int i = 0; i < 8; ++i) t += part[i][threadIdx.x]; o[h * DH + threadIdx.x] = rbf(t); }
}

__global__ void k_wo(const float* o, const u16* wo, const u16* x, const float* mod, float* x1) {
  __shared__ float part[16][64];
  int col = blockIdx.x * 64 + (threadIdx.x & 63), g = threadIdx.x >> 6;
  float acc = 0;
  for (int k = g; k < HQ * DH; k += 16) acc += o[k] * bf2f(wo[(size_t)k * D + col]);
  part[g][threadIdx.x & 63] = acc;
  __syncthreads();
  if (threadIdx.x < 64) { float t = 0; for (int i = 0; i < 16; ++i) t += part[i][threadIdx.x];
    x1[col] = rbf(bf2f(x[col]) + rbf(mod[2 * D + col] * rbf(t))); }
}

__global__ void k_gu(const float* x1, const float* mod, const u16* wg, const u16* wu, float* act) {
  __shared__ float sx[D], sh[D], red[16];
  for (int i = threadIdx.x; i < D; i += blockDim.x) sx[i] = x1[i];
  __syncthreads();
  ln_mod(sx, mod + 4 * D, mod + 3 * D, sh, red);
  int n = blockIdx.x * blockDim.x + threadIdx.x;
  float g = 0, u = 0;
  for (int k = 0; k < D; ++k) { g += sh[k] * bf2f(wg[(size_t)k * F + n]); u += sh[k] * bf2f(wu[(size_t)k * F + n]); }
  g = rbf(g); u = rbf(u);
  g = rbf(g / (1.f + __expf(-g)));
  act[n] = rbf(g * u);
}

__global__ void k_down(const float* act, const u16* wd, const float* x1, const float* mod, u16* out) {
  __shared__ float part[16][64];
  int col = blockIdx.x * 64 + (threadIdx.x & 63), g = threadIdx.x >> 6;
  float acc = 0;
  for (int k = g; k < F; k += 16) acc += act[k] * bf2f(wd[(size_t)k * D + col]);
  part[g][threadIdx.x & 63] = acc;
  __syncthreads();
  if (threadIdx.x < 64) { float t = 0; for (int i = 0; i < 16; ++i) t += part[i][threadIdx.x];
    out[col] = f2bf(x1[col] + rbf(mod[5 * D + col] * rbf(t))); }
}

extern "C" void kernel_launch(void* const* d_in, const int* in_sizes, int n_in,
                              void* d_out, int out_size, void* d_ws, size_t ws_size,
                              hipStream_t stream) {
  const u16* x = (const u16*)d_in[0]; const u16* K = (const u16*)d_in[1]; const u16* V = (const u16*)d_in[2];
  const u16* wq = (const u16*)d_in[3]; const u16* wo = (const u16*)d_in[4];
  const u16* wg = (const u16*)d_in[6]; const u16* wu = (const u16*)d_in[7]; const u16* wd = (const u16*)d_in[8];
  const u16* c = (const u16*)d_in[9]; const u16* wmod = (const u16*)d_in[10];
  float* ws = (float*)d_ws;
  float* mod = ws; float* q = ws + 4096; float* o = ws + 8192; float* x1 = ws + 12288; float* act = ws + 13312; float* sc = ws + 16384;
  k_mod<<<3840 / 64, 64, 0, stream>>>(c, wmod, mod);
  k_q<<<4096 / 64, 64, 0, stream>>>(x, mod, wq, q);
  k_scores<<<dim3(SK / 256, HQ), 256, 0, stream>>>(q, K, sc);
  k_pv<<<HQ, 1024, 0, stream>>>(sc, V, o);
  k_wo<<<D / 64, 1024, 0, stream>>>(o, wo, x, mod, x1);
  k_gu<<<F / 64, 64, 0, stream>>>(x1, mod, wg, wu, act);
  k_down<<<D / 64, 1024, 0, stream>>>(act, wd, x1, mod, (u16*)d_out);
}
```

```cpp
#include <hip/hip_runtime.h>
#include <stdint.h>
#include <stdio.h>

#define DBG_STAMPS 1
#define POL_K 0
#define POL_V 0
#define POL_W 0

namespace adaln {

typedef unsigned short u16;
typedef unsigned long long u64;
typedef unsigned v4u __attribute__((ext_vector_type(4)));
typedef float v2f __attribute__((ext_vector_type(2)));
typedef float v4f __attribute__((ext_vector_type(4)));
typedef __bf16 bf16x2 __attribute__((ext_vector_type(2)));
#define LAS __attribute__((address_space(3)))

constexpr int D = 640, SK = 8192, F = 1728;
constexpr int AUX_NT = 2, AUX_SC1 = 16;
constexpr int RSRC3 = 0x00020000;
constexpr float LOG2E = 1.4426950408889634f;
constexpr unsigned SPIN_CAP = 1u << 16;

constexpr int WS_EPOCH = 0;
constexpr int WS_MOD = 256;
constexpr int WS_Q = WS_MOD + 4 * 1920 * 16;
constexpr int WS_O = WS_Q + 32 * 8 * 64 * 16;
constexpr int WS_Y = WS_O + 32 * 8 * 80 * 16;
constexpr int WS_A = WS_Y + 8 * 10 * 32 * 4 * 16;
constexpr int WS_B = WS_A + 27 * 8 * 64 * 16;
constexpr int WS_C = WS_B + 27 * 8 * 64 * 16;
constexpr int WS_S = WS_C + 27 * 8 * 64 * 16;
constexpr int WS_D = WS_S + 27 * 8 * 128;
constexpr int WS_END = WS_D + 216 * 320 * 16;
constexpr int WS_DBG = 4 << 20;

constexpr int L_Q = 0;
constexpr int L_FLAG = L_Q + 64;
constexpr int L_HS = L_FLAG + 4;
constexpr int L_SC = L_HS + 80;
constexpr int L_TMP = L_SC + 192;
constexpr int L_MOD = L_TMP + 64;
constexpr int L_WM = L_MOD + 480;
constexpr int L_WL = L_WM + 16;
constexpr int L_STAT = L_WL + 16;
constexpr int L_X1 = L_STAT + 32;
constexpr int L_XS = L_X1 + 80;
constexpr int L_OS = L_XS + 80;
constexpr int L_ACT = L_OS + 128;
constexpr int L_PD = L_ACT + 16;
constexpr int L_PART = L_PD + 256;
constexpr int L_PY = L_PART + 2048;
constexpr int L_PA = L_PY + 1280;
constexpr int L_PB = L_PA + 1280;
constexpr int L_PC = L_PB + 1280;
constexpr int L_VT = L_PC + 1280;
static_assert(L_VT % 4 == 0 && L_PART % 4 == 0 && L_VT - L_PART >= 5120, "alignment; room for the wq tile");

__device__ __forceinline__ float bflo(unsigned u) { return __uint_as_float(u << 16); }
__device__ __forceinline__ float bfhi(unsigned u) { return __uint_as_float(u & 0xffff0000u); }
__device__ __forceinline__ float rbf(float f) { float r = (float)(__bf16)f; asm("" : "+v"(r)); return r; }
__device__ __forceinline__ unsigned bfbits(float f) { return (unsigned)__builtin_bit_cast(u16, (__bf16)f); }
__device__ __forceinline__ float ex2(float x) { return __builtin_amdgcn_exp2f(x); }
__device__ __forceinline__ float silu(float x) { return x / (1.f + ex2(-x * LOG2E)); }
__device__ __forceinline__ float rcp(float x) { return __builtin_amdgcn_rcpf(x); }
__device__ __forceinline__ float silu_fast(float x) { return x * rcp(1.f + ex2(-x * LOG2E)); }
__device__ __forceinline__ float dot2(unsigned a, unsigned b, float acc) {
  return __builtin_amdgcn_fdot2_f32_bf16(__builtin_bit_cast(bf16x2, a), __builtin_bit_cast(bf16x2, b), acc, false);
}
template <int CTRL> __device__ __forceinline__ float dpp(float x) {
  return __builtin_bit_cast(float, __builtin_amdgcn_mov_dpp(__builtin_bit_cast(int, x), CTRL, 0xf, 0xf, true));
}
constexpr int XOR1 = 0xB1, XOR2 = 0x4E, HMIR = 0x141, MIR = 0x140, ROR4 = 0x124, ROR8 = 0x128, BCAST = 0x150;
__device__ __forceinline__ float tr32(float a, float b) {
  auto r = __builtin_amdgcn_permlane32_swap(__float_as_uint(a), __float_as_uint(b), false, false);
  return __uint_as_float(r[0]) + __uint_as_float(r[1]);
}
__device__ __forceinline__ float tr16(float a, float b) {
  auto r = __builtin_amdgcn_permlane16_swap(__float_as_uint(a), __float_as_uint(b), false, false);
  return __uint_as_float(r[0]) + __uint_as_float(r[1]);
}
__device__ __forceinline__ float max32(float a) {
  auto r = __builtin_amdgcn_permlane32_swap(__float_as_uint(a), __float_as_uint(a), false, false);
  return fmaxf(__uint_as_float(r[0]), __uint_as_float(r[1]));
}
__device__ __forceinline__ float max16(float a) {
  auto r = __builtin_amdgcn_permlane16_swap(__float_as_uint(a), __float_as_uint(a), false, false);
  return fmaxf(__uint_as_float(r[0]), __uint_as_float(r[1]));
}
template <int CTRL> __device__ __forceinline__ float trd(float a, float b, bool hi) {
  const float t1 = a + dpp<CTRL>(a), t2 = b + dpp<CTRL>(b);
  return hi ? t2 : t1;
}
__device__ __forceinline__ float sum_row16(float x) { x += dpp<XOR1>(x); x += dpp<XOR2>(x); x += dpp<HMIR>(x); x += dpp<MIR>(x); return x; }
__device__ __forceinline__ float max_row16(float x) { x = fmaxf(x, dpp<XOR1>(x)); x = fmaxf(x, dpp<XOR2>(x)); x = fmaxf(x, dpp<HMIR>(x)); return fmaxf(x, dpp<MIR>(x)); }
__device__ __forceinline__ float sum_lo8(float x) { x += dpp<XOR1>(x); x += dpp<XOR2>(x); x += dpp<HMIR>(x); return x; }
__device__ __forceinline__ float max_lo8(float x) { x = fmaxf(x, dpp<XOR1>(x)); x = fmaxf(x, dpp<XOR2>(x)); x = fmaxf(x, dpp<HMIR>(x)); return x; }
__device__ __forceinline__ float sum_hi8(float x) { x += dpp<ROR8>(x); x = tr16(x, x); x = tr32(x, x); return x; }
__device__ __forceinline__ float sum64(float x) { x = sum_row16(x); x = tr16(x, x); return tr32(x, x); }
__device__ __forceinline__ float max64(float x) { return max32(max16(max_row16(x))); }
__device__ __forceinline__ void unpack8(const v4u& w, float (&f)[8]) {
  f[0] = bflo(w.x); f[1] = bfhi(w.x); f[2] = bflo(w.y); f[3] = bfhi(w.y); f[4] = bflo(w.z); f[5] = bfhi(w.z); f[6] = bflo(w.w); f[7] = bfhi(w.w);
}

typedef __amdgpu_buffer_rsrc_t rsrc_t;
__device__ __forceinline__ rsrc_t mk(const void* p, unsigned bytes) { return __builtin_amdgcn_make_buffer_rsrc((void*)p, 0, (int)bytes, RSRC3); }
template <int POL> __device__ __forceinline__ v4u ld_p(rsrc_t r, int voff, int soff) { return __builtin_amdgcn_raw_buffer_load_b128(r, voff, soff, POL); }
#define ld_k ld_p<POL_K>
#define ld_v ld_p<POL_V>
#define ld_w ld_p<POL_W>
__device__ __forceinline__ v4u ld_g(rsrc_t r, int voff, int soff) { return __builtin_amdgcn_raw_buffer_load_b128(r, voff, soff, AUX_SC1); }
__device__ __forceinline__ void st_g(rsrc_t r, int voff, int soff, float a, float b, unsigned tA, unsigned tB) {
  v4u v = {__float_as_uint(a), tA, __float_as_uint(b), tB};
  __builtin_amdgcn_raw_buffer_store_b128(v, r, voff, soff, AUX_SC1);
}
__device__ __forceinline__ bool tag_ok(const v4u& g, unsigned tA, unsigned tB) { return g.y == tA && g.w == tB; }
template <int POL> __device__ __forceinline__ void glds(const void* gsrc, unsigned lds_dst) {
  unsigned keep;
  if (POL == 2)
    asm volatile("s_mov_b32 %0, m0\n\ts_mov_b32 m0, %2\n\ts_nop 0\n\tglobal_load_lds_dwordx4 %1, off nt\n\ts_mov_b32 m0, %0"
                 : "=&s"(keep) : "v"(gsrc), "s"(lds_dst) : "memory");
  else
    asm volatile("s_mov_b32 %0, m0\n\ts_mov_b32 m0, %2\n\ts_nop 0\n\tglobal_load_lds_dwordx4 %1, off\n\ts_mov_b32 m0, %0"
                 : "=&s"(keep) : "v"(gsrc), "s"(lds_dst) : "memory");
}
#define LDS_WAIT() asm volatile("s_waitcnt lgkmcnt(0)" ::: "memory")
#define VM_WAIT(n) asm volatile("s_waitcnt vmcnt(%0)" :: "i"(n) : "memory")
#define BAR() do { asm volatile("s_waitcnt lgkmcnt(0)" ::: "memory"); __builtin_amdgcn_s_barrier(); asm volatile("" ::: "memory"); } while (0)
#define SCHED() __builtin_amdgcn_sched_barrier(0)

template <int N, int SLP, typename OFF>
__device__ __forceinline__ unsigned poll(rsrc_t rs, int voff, OFF soff, bool active, unsigned tA, unsigned tB, v4u (&g)[N]) {
  unsigned spins = 0;
  for (; spins < SPIN_CAP; ++spins) {
    asm volatile("" ::: "memory");
    bool ok = true;
#pragma unroll
    for (int k = 0; k < N; ++k) g[k] = ld_g(rs, voff, soff(k));
#pragma unroll
    for (int k = 0; k < N; ++k) ok = ok && tag_ok(g[k], tA, tB);
    if (__all(ok || !active)) break;
    if (SLP) __builtin_amdgcn_s_sleep(SLP);
  }
  return spins;
}
template <int SLP>
__device__ __forceinline__ unsigned poll2(rsrc_t rs, int voff0, int soff0, int voff1, int soff1, unsigned tA, unsigned tB, v4u& g0, v4u& g1) {
  unsigned spins = 0;
  for (; spins < SPIN_CAP; ++spins) {
    asm volatile("" ::: "memory");
    g0 = ld_g(rs, voff0, soff0);
    g1 = ld_g(rs, voff1, soff1);
    if (__all(tag_ok(g0, tA, tB) && tag_ok(g1, tA, tB))) break;
    if (SLP) __builtin_amdgcn_s_sleep(SLP);
  }
  return spins;
}

__device__ __forceinline__ u64 rt() { return __builtin_amdgcn_s_memrealtime(); }
#define STAMPV(n, v) do { if (lane == (n)) tsv = (u64)(v) + 1; } while (0)
#define STAMP(n) do { const u64 t_ = rt(); if (lane == (n)) tsv = t_; } while (0)

template <int KWIN, int VP, int VL, int PSL, int D1, int D2, int D3, int D4, int TOUCH>
__global__ void __launch_bounds__(1024, 4)
adaln_decode_kernel(const u16* __restrict__ x, const u16* __restrict__ Kc, const u16* __restrict__ Vc, const u16* __restrict__ wq,
                    const u16* __restrict__ wo, const u16* __restrict__ wg, const u16* __restrict__ wu, const u16* __restrict__ wd,
                    const u16* __restrict__ cnd, const u16* __restrict__ wmod, u16* __restrict__ out, unsigned char* __restrict__ ws) {
  constexpr int VR = 16 - VP - VL;
  static_assert(VR >= 0 && VR <= 12, "");
  __shared__ __attribute__((aligned(16))) float lds[L_VT + 16 * (VL > 0 ? VL : 1) * 256];
  const int tid = threadIdx.x, lane = tid & 63, w = __builtin_amdgcn_readfirstlane(tid >> 6);
  const int b = blockIdx.x;
  const int h = (b & 7) * 4 + (b >> 6), s = (b >> 3) & 7;
  const int rho = lane >> 4, c16 = lane & 15;
  u64 tsv = 0;
  STAMP(0);
  const u64 clk0 = __builtin_amdgcn_s_memtime();
  volatile LAS unsigned* qflag = (volatile LAS unsigned*)&lds[L_FLAG];
  if (tid == 0) *qflag = 0u;
  const rsrc_t rws = mk(ws, WS_END);
  const size_t kvbase = ((size_t)h * SK + s * 1024) * 256;
  const rsrc_t rk = mk((const char*)Kc + kvbase, 262144);
  const rsrc_t rv = mk((const char*)Vc + kvbase, 262144);
  const int rot = ((b >> 3) * 5 + (b & 7) * 3) & 15;
  auto toff = [&](int i) { return ((((i + rot) & 15) << 4) + w) << 10; };
  const uint2 ep = *(const uint2*)(ws + WS_EPOCH);
  const unsigned tA = ep.x + 1u, tB = ep.y + 1u;
  BAR();

  v4u kv[16], vp[VP > 0 ? VP : 1];
  const unsigned vt_lds = (unsigned)(uintptr_t)(LAS float*)&lds[L_VT] + (unsigned)w * (VL * 1024);
  const char* vgl = (const char*)Vc + kvbase + lane * 16;
  if (w == 0) {
    const int cb = b % 60, rq = b / 60;
    const bool has_mod = b < 240;
    {
      const rsrc_t rm = mk((const char*)wmod + (size_t)(has_mod ? rq : 0) * 160 * 7680 + (has_mod ? cb : 0) * 128, has_mod ? 160 * 7680 : 0);
      v4u t[20];
      const int voff = (lane >> 3) * 7680 + (lane & 7) * 16;
      const int r0 = (has_mod ? rq : 0) * 160;
      u16 cv[3];
#pragma unroll
      for (int k = 0; k < 3; ++k) { const int i = lane + 64 * k; cv[k] = cnd[r0 + (i < 160 ? i : 0)]; }
#pragma unroll
      for (int u = 0; u < 20; ++u) t[u] = ld_w(rm, voff, u * 8 * 7680);
      SCHED();
#pragma unroll
      for (int k = 0; k < 3; ++k) lds[L_SC + lane + 64 * k] = rbf(silu(bflo(cv[k])));
      LDS_WAIT();
      float acc[8];
#pragma unroll
      for (int j = 0; j < 8; ++j) acc[j] = 0.f;
#pragma unroll
      for (int u = 0; u < 20; ++u) {
        const float sv = lds[L_SC + 8 * u + (lane >> 3)];
        float f[8]; unpack8(t[u], f);
#pragma unroll
        for (int j = 0; j < 8; ++j) acc[j] = fmaf(sv, f[j], acc[j]);
      }
      float y[4];
#pragma unroll
      for (int j = 0; j < 4; ++j) y[j] = tr32(acc[j], acc[j + 4]);
      float z0 = tr16(y[0], y[2]), z1 = tr16(y[1], y[3]);
      z0 += dpp<ROR8>(z0); z1 += dpp<ROR8>(z1);
      const int gi = 4 * (lane & 7) + rho;
      if ((lane & 8) == 0) *(LAS v2f*)&lds[L_TMP + 2 * gi] = v2f{z0, z1};
      LDS_WAIT();
      if (has_mod && lane < 32) { const v2f v = *(LAS v2f*)&lds[L_TMP + 2 * lane]; st_g(rws, lane * 16, WS_MOD + (rq * 1920 + cb * 32) * 16, v.x, v.y, tA, tB); }
    }
    STAMP(1);
    {
      const char* gq = (const char*)wq + (size_t)(s * 80 + rho) * 8192 + h * 256 + c16 * 16;
      const unsigned dst = (unsigned)(uintptr_t)(LAS float*)&lds[L_PART];
#pragma unroll
      for (int u = 0; u < 20; ++u) glds<POL_W>(gq + (size_t)u * 4 * 8192, dst + u * 1024);
    }
    SCHED();
    float mean_b, den_b;
    {
      float xv[10];
#pragma unroll
      for (int k = 0; k < 5; ++k) { const unsigned u = ((const unsigned*)x)[lane + 64 * k]; xv[2 * k] = bflo(u); xv[2 * k + 1] = bfhi(u); }
      float sm = 0.f;
#pragma unroll
      for (int k = 0; k < 10; ++k) sm += xv[k];
      const float mean = sum64(sm) * (1.f / D);
      float vs = 0.f;
#pragma unroll
      for (int k = 0; k < 10; ++k) { const float d = xv[k] - mean; vs = fmaf(d, d, vs); }
      const float var = sum64(vs) * (1.f / D);
      mean_b = rbf(mean); den_b = rbf(sqrtf(rbf(rbf(var) + 1e-5f)));
    }
    const int j40 = lane < 40 ? lane : 0;
    const unsigned xs2 = ((const unsigned*)x)[40 * s + j40];
    if (lane < 40) *(LAS v2f*)&lds[L_XS + 2 * lane] = v2f{bflo(xs2), bfhi(xs2)};
    {
      v4u g[8];
      poll<8, PSL>(rws, j40 * 16, [&](int k) { return WS_MOD + ((k & 3) * 1920 + (k >> 2) * 320 + 40 * s) * 16; }, lane < 40, tA, tB, g);
      const float sa0 = rbf((__uint_as_float(g[0].x) + __uint_as_float(g[1].x)) + (__uint_as_float(g[2].x) + __uint_as_float(g[3].x)));
      const float sa1 = rbf((__uint_as_float(g[0].z) + __uint_as_float(g[1].z)) + (__uint_as_float(g[2].z) + __uint_as_float(g[3].z)));
      const float ca0 = rbf((__uint_as_float(g[4].x) + __uint_as_float(g[5].x)) + (__uint_as_float(g[6].x) + __uint_as_float(g[7].x)));
      const float ca1 = rbf((__uint_as_float(g[4].z) + __uint_as_float(g[5].z)) + (__uint_as_float(g[6].z) + __uint_as_float(g[7].z)));
      const float h0 = rbf(rbf(rbf(rbf(bflo(xs2) - mean_b) / den_b) * rbf(1.f + ca0)) + sa0);
      const float h1 = rbf(rbf(rbf(rbf(bfhi(xs2) - mean_b) / den_b) * rbf(1.f + ca1)) + sa1);
      if (lane < 40) *(LAS v2f*)&lds[L_HS + 2 * lane] = v2f{h0, h1};
      VM_WAIT(0);
      LDS_WAIT();
    }
    STAMP(2);
    {
      float acc[8];
#pragma unroll
      for (int j = 0; j < 8; ++j) acc[j] = 0.f;
#pragma unroll
      for (int u = 0; u < 20; ++u) {
        const float hv = lds[L_HS + 4 * u + rho];
        const v4u tq = *(LAS v4u*)&lds[L_PART + u * 256 + lane * 4];
        float f[8]; unpack8(tq, f);
#pragma unroll
        for (int j = 0; j < 8; ++j) acc[j] = fmaf(hv, f[j], acc[j]);
      }
      float y[4];
#pragma unroll
      for (int j = 0; j < 4; ++j) y[j] = tr32(acc[j], acc[j + 4]);
      const float z0 = tr16(y[0], y[2]), z1 = tr16(y[1], y[3]);
      st_g(rws, lane * 16, WS_Q + (h * 8 + s) * 1024, z0, z1, tA, tB);
    }
    STAMP(3);
    {
      v4u g[8];
      poll<8, 0>(rws, lane * 16, [&](int k) { return WS_Q + (h * 8 + k) * 1024; }, true, tA, tB, g);
      float q0 = 0.f, q1 = 0.f;
#pragma unroll
      for (int k = 0; k < 8; ++k) { q0 += __uint_as_float(g[k].x); q1 += __uint_as_float(g[k].z); }
      ((LAS unsigned*)lds)[L_Q + 4 * c16 + rho] = bfbits(q0) | (bfbits(q1) << 16);
    }
    LDS_WAIT();
    *qflag = 1u;
    STAMP(4);
#pragma unroll
    for (int i = 0; i < 16; ++i) kv[i] = ld_k(rk, lane * 16, toff(i));
    SCHED();
#pragma unroll
    for (int i = 0; i < VP; ++i) vp[i] = ld_v(rv, lane * 16, toff(i));
#pragma unroll
    for (int i = 0; i < VL; ++i) glds<POL_V>(vgl + toff(VP + i), vt_lds + i * 1024);
    SCHED();
#pragma unroll
    for (int ch = 2; ch < 6; ++ch) {
      v4u g[4];
      poll<4, 1>(rws, j40 * 16, [&](int k) { return WS_MOD + (k * 1920 + ch * 320 + 40 * s) * 16; }, lane < 40, tA, tB, g);
      const float m0 = rbf((__uint_as_float(g[0].x) + __uint_as_float(g[1].x)) + (__uint_as_float(g[2].x) + __uint_as_float(g[3].x)));
      const float m1 = rbf((__uint_as_float(g[0].z) + __uint_as_float(g[1].z)) + (__uint_as_float(g[2].z) + __uint_as_float(g[3].z)));
      if (lane < 40) *(LAS v2f*)&lds[L_MOD + ch * 80 + 2 * lane] = v2f{m0, m1};
    }
    VM_WAIT(0);
  } else {
    if (TOUCH && w == 15) {
      const int g_ = h < 27 ? h : 0;
      int o0;
      if (lane < 9) o0 = WS_O + (h * 8 + s) * 1280 + lane * 128;
      else if (lane < 19) o0 = WS_Y + (s * 10 * 32 + h) * 64 + (lane - 9) * 2048;
      else if (lane < 27) o0 = WS_A + (g_ * 512 + s * 8) * 16 + (lane - 19) * 1024;
      else if (lane < 35) o0 = WS_B + (g_ * 512 + s * 8) * 16 + (lane - 27) * 1024;
      else if (lane < 43) o0 = WS_C + (g_ * 512 + s * 8) * 16 + (lane - 35) * 1024;
      else o0 = WS_S + (g_ * 8 + s) * 128;
      const unsigned t0_ = __builtin_amdgcn_raw_buffer_load_b32(rws, o0, 0, TOUCH == 1 ? AUX_SC1 : 0);
      const unsigned t1_ = __builtin_amdgcn_raw_buffer_load_b32(rws, (lane < 40 ? lane : 0) * 128, WS_D + (g_ * 8 + s) * 5120, TOUCH == 1 ? AUX_SC1 : 0);
      asm volatile("" :: "v"(t0_), "v"(t1_));
    }
#pragma unroll
    for (int i = 0; i < 16; ++i) {
      kv[i] = ld_k(rk, lane * 16, toff(i));
      if (i >= KWIN - 1) VM_WAIT(KWIN - 1);
      SCHED();
    }
#pragma unroll
    for (int i = 0; i < VP; ++i) { vp[i] = ld_v(rv, lane * 16, toff(i)); VM_WAIT(KWIN - 1); SCHED(); }
#pragma unroll
    for (int i = 0; i < VL; ++i) { glds<POL_V>(vgl + toff(VP + i), vt_lds + i * 1024); if (i < VL - 1) VM_WAIT(KWIN - 1); SCHED(); }
    STAMP(4);
    for (unsigned spins = 0; spins < (SPIN_CAP << 6); ++spins) { if (*qflag != 0u) break; __builtin_amdgcn_s_sleep(1); }
    asm volatile("" ::: "memory");
  }
  STAMP(5);
  const bool ffn = h < 27;
  const bool gw = ffn && w < 10, dw = ffn && w >= 10 && w < 15;
  const rsrc_t ro = mk((const char*)wo + (size_t)(h * 128 + 8 * w) * 1280 + s * 160, 8 * 1280);
  const int voff_o = c16 < 10 ? rho * 1280 + c16 * 16 : 0x7fffff00;
  const size_t goff = (size_t)(80 * s + 8 * (gw ? w : 0)) * 3456 + (gw ? h : 0) * 128;
  const rsrc_t rf0 = gw ? mk((const char*)wg + goff, 8 * 3456) : mk((const char*)wd + (size_t)(dw ? 64 * h + 8 * s : 0) * 1280, dw ? 8 * 1280 : 0);
  const rsrc_t rf1 = gw ? mk((const char*)wu + goff, 8 * 3456) : mk((const char*)wd + (size_t)(dw ? 64 * h + 8 * s : 0) * 1280 + 1280, dw ? 7 * 1280 : 0);
  const int voff_f = gw ? (lane >> 3) * 3456 + (lane & 7) * 16 : (lane & 3) * 2560 + ((dw ? w - 10 : 0) * 16 + (lane >> 2)) * 16;
  v4u wot[2], fwt[2];
  const v4u qf = *(LAS v4u*)&lds[L_Q + 4 * c16];
  float sc;
  {
    float xk[16];
#pragma unroll
    for (int i = 0; i < 16; ++i) {
      float d = dot2(kv[i].x, qf.x, 0.f); d = dot2(kv[i].y, qf.y, d); d = dot2(kv[i].z, qf.z, d); d = dot2(kv[i].w, qf.w, d);
      xk[i] = d;
      asm volatile("" : "+v"(xk[i]));
      if (i == 0) wot[0] = ld_w(ro, voff_o, 0);
      if (i == 1) wot[1] = ld_w(ro, voff_o, 4 * 1280);
      SCHED();
    }
#pragma unroll
    for (int j = 0; j < 8; ++j) xk[j] = trd<ROR8>(xk[j], xk[j + 8], (lane & 8) != 0);
#pragma unroll
    for (int j = 0; j < 4; ++j) xk[j] = trd<HMIR>(xk[j], xk[j + 4], (lane & 4) != 0);
#pragma unroll
    for (int j = 0; j < 2; ++j) xk[j] = trd<XOR2>(xk[j], xk[j + 2], (lane & 2) != 0);
    const float dot = trd<XOR1>(xk[0], xk[1], (lane & 1) != 0);
    sc = rbf(rbf(dot) * 0.08838834764831845f);
  }
  const float mw = max64(sc);
  const float p = ex2((sc - mw) * LOG2E);
  { const float lw = sum64(p); if (lane == 0) { lds[L_WM + w] = mw; lds[L_WL + w] = lw; } }
  STAMP(6);
  float acc[8];
#pragma unroll
  for (int j = 0; j < 8; ++j) acc[j] = 0.f;
#define PV_ACC(i, reg) { const float pb = dpp<BCAST + (i)>(p); float f[8]; unpack8(reg, f); \
    _Pragma("unroll") for (int j = 0; j < 8; ++j) acc[j] = fmaf(pb, f[j], acc[j]); }
  VM_WAIT(1); fwt[0] = ld_w(rf0, voff_f, 0); SCHED();
  VM_WAIT(1); fwt[1] = ld_w(rf1, voff_f, 0); SCHED();
#define PV_LANDED(i) if ((i) < VP) PV_ACC(i, vp[(i) < VP ? (i) : 0]) \
    else if ((i) < VP + VL) { const v4u t_ = *(LAS v4u*)&lds[L_VT + (w * VL + ((i) - VP)) * 256 + lane * 4]; PV_ACC(i, t_) }
  PV_LANDED(0) PV_LANDED(1) PV_LANDED(2) PV_LANDED(3) PV_LANDED(4) PV_LANDED(5) PV_LANDED(6) PV_LANDED(7)
  PV_LANDED(8) PV_LANDED(9) PV_LANDED(10) PV_LANDED(11) PV_LANDED(12) PV_LANDED(13) PV_LANDED(14) PV_LANDED(15)
#undef PV_LANDED
  SCHED();
#pragma unroll
  for (int i = 0; i < VR; ++i) { VM_WAIT(KWIN - 1); kv[i] = ld_v(rv, lane * 16, toff(VP + VL + i)); SCHED(); }
#define PV_LATE(i) if ((i) >= VP + VL) PV_ACC(i, kv[(i) >= VP + VL ? (i) - VP - VL : 0])
  PV_LATE(0) PV_LATE(1) PV_LATE(2) PV_LATE(3) PV_LATE(4) PV_LATE(5) PV_LATE(6) PV_LATE(7)
  PV_LATE(8) PV_LATE(9) PV_LATE(10) PV_LATE(11) PV_LATE(12) PV_LATE(13) PV_LATE(14) PV_LATE(15)
#undef PV_LATE
#undef PV_ACC
  STAMP(7);
  {
    float y[4];
#pragma unroll
    for (int j = 0; j < 4; ++j) y[j] = tr32(acc[j], acc[j + 4]);
    const float z0 = tr16(y[0], y[2]), z1 = tr16(y[1], y[3]);
    *(LAS v2f*)&lds[L_PART + w * 128 + 8 * c16 + 2 * rho] = v2f{z0, z1};
  }
  BAR();
  STAMP(8);
  const int r8 = lane >> 3;
  float gf[8], uf[8];
  float bmul = 0.f;
  if (w == 15) {
    __builtin_amdgcn_s_setprio(3);
    const int hi = lane >> 5;
    const float wm_a = lds[L_WM + c16], wm_b = lds[L_WM + (c16 & 7) + 8 * hi], wl_a = lds[L_WL + c16];
    v4f pv[8];
#pragma unroll
    for (int k = 0; k < 8; ++k) pv[k] = *(LAS v4f*)&lds[L_PART + (k + 8 * hi) * 128 + 4 * (lane & 31)];
    SCHED();
    const float Mcu = max_row16(wm_a);
    const float fk = ex2((wm_b - Mcu) * LOG2E);
    const float Lcu = sum_row16(ex2((wm_a - Mcu) * LOG2E) * wl_a);
    v4f o4 = {0.f, 0.f, 0.f, 0.f};
#define MRG(k) { const float f = dpp<BCAST + (k)>(fk); o4 += f * pv[k]; }
    MRG(0) MRG(1) MRG(2) MRG(3) MRG(4) MRG(5) MRG(6) MRG(7)
#undef MRG
    st_g(rws, lane * 16, WS_O + (h * 8 + s) * 1280, tr32(o4.x, o4.z), tr32(o4.y, o4.w), tA, tB);
    if (lane == 0) st_g(rws, 64 * 16, WS_O + (h * 8 + s) * 1280, Mcu, Lcu, tA, tB);
    STAMP(9);
    if (D1) __builtin_amdgcn_s_sleep(D1);
    v4u go[8], gml;
    for (unsigned spins = 0; spins < SPIN_CAP; ++spins) {
      asm volatile("" ::: "memory");
#pragma unroll
      for (int k = 0; k < 8; ++k) go[k] = ld_g(rws, lane * 16, WS_O + (h * 8 + k) * 1280);
      gml = ld_g(rws, (lane & 7) * 1280 + 64 * 16, WS_O + h * 8 * 1280);
      unsigned bad = (gml.y ^ tA) | (gml.w ^ tB);
#pragma unroll
      for (int k = 0; k < 8; ++k) bad |= (go[k].y ^ tA) | (go[k].w ^ tB);
      if (__all(bad == 0u)) break;
      __builtin_amdgcn_s_sleep(2);
    }
    STAMP(10);
    const float m = __uint_as_float(gml.x), l = __uint_as_float(gml.z);
    const float M = max_lo8(m);
    const float f = ex2((m - M) * LOG2E);
    const float rL = rcp(sum_lo8(f * l));
    float o0 = 0.f, o1 = 0.f;
#define CMB(k) { const float fb = __uint_as_float(__builtin_amdgcn_readlane(__float_as_uint(f), k)); o0 = fmaf(fb, __uint_as_float(go[k].x), o0); o1 = fmaf(fb, __uint_as_float(go[k].z), o1); }
    CMB(0) CMB(1) CMB(2) CMB(3) CMB(4) CMB(5) CMB(6) CMB(7)
#undef CMB
    *(LAS v2f*)&lds[L_OS + 4 * (lane & 31) + 2 * hi] = v2f{rbf(o0 * rL), rbf(o1 * rL)};
  } else if (gw) {
    unpack8(fwt[0], gf); unpack8(fwt[1], uf);
    bmul = rbf(1.f + lds[L_MOD + 4 * 80 + 8 * w + r8]);
    const float cmul = lds[L_MOD + 3 * 80 + 8 * w + r8];
#pragma unroll
    for (int t = 0; t < 2; ++t) {
      const float mul = t == 0 ? bmul : cmul;
      float y[8];
#pragma unroll
      for (int j = 0; j < 8; ++j) y[j] = tr32(mul * gf[j], mul * uf[j]);
      float z[4];
#pragma unroll
      for (int j = 0; j < 4; ++j) z[j] = tr16(y[j], y[j + 4]);
      const float e0 = trd<ROR8>(z[0], z[2], (lane & 8) != 0), e1 = trd<ROR8>(z[1], z[3], (lane & 8) != 0);
      *(LAS v2f*)&lds[(t == 0 ? L_PB : L_PC) + w * 128 + (lane >> 5) * 64 + 8 * (lane & 7) + 4 * ((lane >> 4) & 1) + 2 * ((lane >> 3) & 1)] = v2f{e0, e1};
    }
  }
  BAR();
  STAMP(11);
  {
    const float o0 = lds[L_OS + 8 * w + rho], o1 = lds[L_OS + 8 * w + 4 + rho];
    float f0[8], f1[8]; unpack8(wot[0], f0); unpack8(wot[1], f1);
    float a[8];
#pragma unroll
    for (int j = 0; j < 8; ++j) a[j] = fmaf(o0, f0[j], o1 * f1[j]);
    float y[4];
#pragma unroll
    for (int j = 0; j < 4; ++j) y[j] = tr32(a[j], a[j + 4]);
    const float z0 = tr16(y[0], y[2]), z1 = tr16(y[1], y[3]);
    if (c16 < 10) *(LAS v2f*)&lds[L_PY + w * 80 + 8 * c16 + 2 * rho] = v2f{z0, z1};
  }
  STAMP(12);
  BAR();
  STAMP(13);
  const int j40 = lane < 40 ? lane : 0;
  if (w == 15) {
    v2f pv[16];
#pragma unroll
    for (int k = 0; k < 16; ++k) pv[k] = *(LAS v2f*)&lds[L_PY + k * 80 + 2 * j40];
    SCHED();
    v2f ya = (pv[0] + pv[1]) + (pv[2] + pv[3]), yb = (pv[4] + pv[5]) + (pv[6] + pv[7]), yc = (pv[8] + pv[9]) + (pv[10] + pv[11]), yd = (pv[12] + pv[13]) + (pv[14] + pv[15]);
    ya = (ya + yb) + (yc + yd);
    const float y0 = ya.x, y1 = ya.y;
    if (lane < 40) st_g(rws, ((lane >> 2) * 32 * 4 + (lane & 3)) * 16, WS_Y + (s * 10 * 32 + h) * 64, y0, y1, tA, tB);
    STAMP(14);
  }
  if (ffn) {
    const int g = h, r = s;
    const int sig = ((lane & 7) >> 1) | ((lane & 1) << 2);
    if (w == 10 || w == 11) {
      const int pb = w == 10 ? L_PB : L_PC;
      float a0 = 0.f, a1 = 0.f;
#pragma unroll
      for (int k = 0; k < 10; ++k) { a0 += lds[pb + k * 128 + lane]; a1 += lds[pb + k * 128 + 64 + lane]; }
      st_g(rws, ((lane >> 3) * 64 + sig) * 16, (w == 10 ? WS_B : WS_C) + (g * 512 + r * 8) * 16, a0, a1, tA, tB);
    }
    float Bg = 0.f, Bu = 0.f, Cg = 0.f, Cu = 0.f;
    if (w == 10) {
      __builtin_amdgcn_s_sleep(16);
      v4u gb[2];
      poll<2, 8>(rws, lane * 16, [&](int k) { return (k == 0 ? WS_B : WS_C) + (g * 8 + r) * 1024; }, true, tA, tB, gb);
      Bg = sum_hi8(__uint_as_float(gb[0].x)); Bu = sum_hi8(__uint_as_float(gb[0].z));
      Cg = sum_hi8(__uint_as_float(gb[1].x)); Cu = sum_hi8(__uint_as_float(gb[1].z));
    }
    if (w < 10) {
      if (D2) __builtin_amdgcn_s_sleep(D2);
      v4u gy[2];
      const unsigned np2 = poll<2, PSL>(rws, c16 * 64 + rho * 16, [&](int k) { return WS_Y + ((r * 10 + w) * 32 + 16 * k) * 64; }, true, tA, tB, gy);
      STAMPV(26, np2);
      STAMP(16);
      const float y0 = sum_row16(__uint_as_float(gy[0].x) + __uint_as_float(gy[1].x));
      const float y1 = sum_row16(__uint_as_float(gy[0].z) + __uint_as_float(gy[1].z));
      const float attn = rbf((lane & 8) ? y1 : y0);
      const int cl = 8 * w + r8;
      const float x1 = rbf(lds[L_XS + cl] + rbf(lds[L_MOD + 2 * 80 + cl] * attn));
      const float xs = x1 * bmul;
      float y[8];
#pragma unroll
      for (int j = 0; j < 8; ++j) y[j] = tr32(xs * gf[j], xs * uf[j]);
      float z[4];
#pragma unroll
      for (int j = 0; j < 4; ++j) z[j] = tr16(y[j], y[j + 4]);
      const float e0 = trd<ROR8>(z[0], z[2], (lane & 8) != 0), e1 = trd<ROR8>(z[1], z[3], (lane & 8) != 0);
      *(LAS v2f*)&lds[L_PA + w * 128 + (lane >> 5) * 64 + 8 * (lane & 7) + 4 * ((lane >> 4) & 1) + 2 * ((lane >> 3) & 1)] = v2f{e0, e1};
      const float mean_w = sum_hi8(x1) * 0.125f;
      const float dd = x1 - mean_w;
      const float m2_w = sum_hi8(dd * dd);
      if (lane == 0) *(LAS v2f*)&lds[L_STAT + 2 * w] = v2f{mean_w, m2_w};
      if ((lane & 7) == 0) lds[L_X1 + cl] = x1;
    }
    STAMP(17);
    BAR();
    STAMP(18);
    if (w == 0) {
      float pa[10], pb_[10];
#pragma unroll
      for (int k = 0; k < 10; ++k) { pa[k] = lds[L_PA + k * 128 + lane]; pb_[k] = lds[L_PA + k * 128 + 64 + lane]; }
      SCHED();
      const float a0 = ((pa[0] + pa[1]) + (pa[2] + pa[3])) + ((pa[4] + pa[5]) + (pa[6] + pa[7])) + (pa[8] + pa[9]);
      const float a1 = ((pb_[0] + pb_[1]) + (pb_[2] + pb_[3])) + ((pb_[4] + pb_[5]) + (pb_[6] + pb_[7])) + (pb_[8] + pb_[9]);
      st_g(rws, ((lane >> 3) * 64 + sig) * 16, WS_A + (g * 512 + r * 8) * 16, a0, a1, tA, tB);
      STAMP(19);
    } else if (w == 1) {
      const v2f sv = *(LAS v2f*)&lds[L_STAT + 2 * (c16 < 10 ? c16 : 0)];
      const float mk_ = c16 < 10 ? sv.x : 0.f;
      const float mean_r = sum_row16(mk_) * 0.1f;
      const float dd = sv.x - mean_r;
      const float m2 = sum_row16(c16 < 10 ? fmaf(8.f * dd, dd, sv.y) : 0.f);
      if (lane == 0) st_g(rws, 0, WS_S + (g * 8 + r) * 128, mean_r, m2, tA, tB);
      STAMP(15);
    } else if (w == 10) {
      if (D3) __builtin_amdgcn_s_sleep(D3);
      v4u ga, gs;
      STAMP(25);
      const unsigned np3 = poll2<1>(rws, lane * 16, WS_A + (g * 8 + r) * 1024, (lane >> 3) * 128, WS_S + g * 1024, tA, tB, ga, gs);
      STAMPV(27, np3);
      STAMP(20);
      const float Ag = sum_hi8(__uint_as_float(ga.x)), Au = sum_hi8(__uint_as_float(ga.z));
      const float mr = __uint_as_float(gs.x);
      const float mean = sum_hi8(mr) * 0.125f;
      const float dd = mr - mean;
      const float var = sum_hi8(fmaf(80.f * dd, dd, __uint_as_float(gs.z))) * (1.f / D);
      const float mb = rbf(mean), den = rbf(__builtin_amdgcn_sqrtf(rbf(rbf(var) + 1e-5f)));
      const float rden = rcp(den);
      const float gate = rbf(fmaf(Ag - mb * Bg, rden, Cg)), up = rbf(fmaf(Au - mb * Bu, rden, Cu));
      const float act = rbf(rbf(silu_fast(gate)) * up);
      if (lane < 8) lds[L_ACT + 2 * (lane & 3) + (lane >> 2)] = act;
    }
    BAR();
    STAMP(21);
    if (dw) {
      const v2f av = *(LAS v2f*)&lds[L_ACT + 2 * (lane & 3)];
      float f0[8], f1[8]; unpack8(fwt[0], f0); unpack8(fwt[1], f1);
      float d[8];
#pragma unroll
      for (int j = 0; j < 8; ++j) d[j] = fmaf(av.x, f0[j], av.y * f1[j]);
      float y[4];
#pragma unroll
      for (int j = 0; j < 4; ++j) y[j] = trd<XOR2>(d[j], d[j + 4], (lane & 2) != 0);
      const float e0 = trd<XOR1>(y[0], y[2], (lane & 1) != 0), e1 = trd<XOR1>(y[1], y[3], (lane & 1) != 0);
      st_g(rws, lane * 16, WS_D + ((g * 8 + r) * 320 + (w - 10) * 64) * 16, e0, e1, tA, tB);
      STAMP(22);
    }
    if (g < 5) {
      if (D4) __builtin_amdgcn_s_sleep(D4);
      v4u gd[2];
      const int n1 = w + 16 < 27 ? w + 16 : w;
      const int voff = (lane >> 3) * 5120 + (lane & 7) * 16;
      STAMP(23);
      const unsigned np4 = poll<2, 1>(rws, voff, [&](int k) { return WS_D + (k == 0 ? w : n1) * 8 * 5120 + (40 * r + 8 * g) * 16; }, true, tA, tB, gd);
      STAMPV(28, np4);
      STAMP(24);
      float e0 = __uint_as_float(gd[0].x), e1 = __uint_as_float(gd[0].z);
      if (w + 16 < 27) { e0 += __uint_as_float(gd[1].x); e1 += __uint_as_float(gd[1].z); }
      e0 = sum_hi8(e0); e1 = sum_hi8(e1);
      if (lane < 8) *(LAS v2f*)&lds[L_PD + w * 16 + 2 * lane] = v2f{e0, e1};
      BAR();
      if (w == 0 && lane < 8) {
        v2f pv[16];
#pragma unroll
        for (int k = 0; k < 16; ++k) pv[k] = *(LAS v2f*)&lds[L_PD + k * 16 + 2 * lane];
        const int cl = 16 * g + 2 * lane;
        const v2f x1v = *(LAS v2f*)&lds[L_X1 + cl], gbv = *(LAS v2f*)&lds[L_MOD + 5 * 80 + cl];
        SCHED();
        v2f ya = (pv[0] + pv[1]) + (pv[2] + pv[3]), yb = (pv[4] + pv[5]) + (pv[6] + pv[7]), yc = (pv[8] + pv[9]) + (pv[10] + pv[11]), yd = (pv[12] + pv[13]) + (pv[14] + pv[15]);
        ya = (ya + yb) + (yc + yd);
        const float f0 = ya.x, f1 = ya.y;
        const float o0 = x1v.x + rbf(gbv.x * rbf(f0));
        const float o1 = x1v.y + rbf(gbv.y * rbf(f1));
        ((unsigned*)out)[(80 * r + cl) >> 1] = bfbits(o0) | (bfbits(o1) << 16);
        if (b == 0 && lane == 0) *(uint2*)(ws + WS_EPOCH) = make_uint2(tA, tB);
      }
    }
  }
  STAMP(30);
  { const u64 dc = __builtin_amdgcn_s_memtime() - clk0; if (lane == 31) tsv = dc; }
  if (lane < 32) ((u64*)(ws + WS_DBG))[(size_t)(b * 16 + w) * 32 + lane] = tsv;
}

template <int KWIN, int VP, int VL, int PSL, int D1, int D2, int D3, int D4, int TOUCH>
static void launch(void* const* d_in, void* d_out, void* d_ws, hipStream_t stream) {
  adaln_decode_kernel<KWIN, VP, VL, PSL, D1, D2, D3, D4, TOUCH><<<256, 1024, 0, stream>>>(
      (const u16*)d_in[0], (const u16*)d_in[1], (const u16*)d_in[2], (const u16*)d_in[3], (const u16*)d_in[4], (const u16*)d_in[6],
      (const u16*)d_in[7], (const u16*)d_in[8], (const u16*)d_in[9], (const u16*)d_in[10], (u16*)d_out, (unsigned char*)d_ws);
}

__global__ void k_report(const unsigned char* ws) {
  __shared__ unsigned long long mn, mx[32];
  const u64* hs = (const u64*)(ws + WS_DBG);
  if (threadIdx.x == 0) mn = ~0ull;
  if (threadIdx.x < 32) mx[threadIdx.x] = 0;
  __syncthreads();
  for (int i = threadIdx.x; i < 4096; i += blockDim.x) atomicMin(&mn, hs[i * 32]);
  __syncthreads();
  for (int i = threadIdx.x; i < 4096; i += blockDim.x) for (int n = 0; n < 31; ++n) { u64 v = hs[i * 32 + n]; if (v > mn && (n < 26 || n > 28)) atomicMax(&mx[n], v - mn); }
  __syncthreads();
  if (threadIdx.x == 0) { printf("STAMPS max:"); for (int n = 0; n < 31; ++n) if (mx[n]) printf(" %d:%.2f", n, mx[n] * 0.01); printf("  clk %.0f cyc over %.2f us\n", (double)hs[31], (hs[30] - hs[0]) * 0.01);
    printf("ws ptr %p\n", ws); }
}

}

extern "C" void kernel_launch(void* const* d_in, const int* in_sizes, int n_in, void* d_out, int out_size, void* d_ws, size_t ws_size,
                              hipStream_t stream) {
  adaln::launch<2, 6, 6, 8, 8, 8, 8, 8, 2>(d_in, d_out, d_ws, stream);
  adaln::k_report<<<1, 1024, 0, stream>>>((const unsigned char*)d_ws);
  fprintf(stderr, "ptrs: x %p K %p V %p wq %p wo %p wg %p wmod %p out %p ws %p\n", d_in[0], d_in[1], d_in[2], d_in[3], d_in[4], d_in[6], d_in[10], d_out, d_ws);
}
```

```cpp
#include <hip/hip_runtime.h>
#include <stdint.h>

#define DBG_STAMPS 1
#define POL_K 2
#define POL_V 2
#define POL_W 2

namespace adaln {

typedef unsigned short u16;
typedef unsigned long long u64;
typedef unsigned v4u __attribute__((ext_vector_type(4)));
typedef float v2f __attribute__((ext_vector_type(2)));
typedef float v4f __attribute__((ext_vector_type(4)));
typedef __bf16 bf16x2 __attribute__((ext_vector_type(2)));
#define LAS __attribute__((address_space(3)))

constexpr int D = 640, SK = 8192, F = 1728;
constexpr int AUX_NT = 2, AUX_SC1 = 16;
constexpr int RSRC3 = 0x00020000;
constexpr float LOG2E = 1.4426950408889634f;
constexpr unsigned SPIN_CAP = 1u << 16;

constexpr int WS_EPOCH = 0;
constexpr int WS_MOD = 256;
constexpr int WS_Q = WS_MOD + 4 * 1920 * 16;
constexpr int WS_O = WS_Q + 32 * 8 * 64 * 16;
constexpr int WS_Y = WS_O + 32 * 8 * 80 * 16;
constexpr int WS_A = WS_Y + 8 * 10 * 32 * 4 * 16;
constexpr int WS_B = WS_A + 27 * 8 * 64 * 16;
constexpr int WS_C = WS_B + 27 * 8 * 64 * 16;
constexpr int WS_S = WS_C + 27 * 8 * 64 * 16;
constexpr int WS_D = WS_S + 27 * 8 * 128;
constexpr int WS_END = WS_D + 216 * 320 * 16;
constexpr int WS_DBG = 4 << 20;

constexpr int L_Q = 0;
constexpr int L_FLAG = L_Q + 64;
constexpr int L_HS = L_FLAG + 4;
constexpr int L_SC = L_HS + 80;
constexpr int L_TMP = L_SC + 192;
constexpr int L_MOD = L_TMP + 64;
constexpr int L_WM = L_MOD + 480;
constexpr int L_WL = L_WM + 16;
constexpr int L_STAT = L_WL + 16;
constexpr int L_X1 = L_STAT + 32;
constexpr int L_XS = L_X1 + 80;
constexpr int L_OS = L_XS + 80;
constexpr int L_ACT = L_OS + 128;
constexpr int L_PD = L_ACT + 16;
constexpr int L_PART = L_PD + 256;
constexpr int L_PY = L_PART + 2048;
constexpr int L_PA = L_PY + 1280;
constexpr int L_PB = L_PA + 1280;
constexpr int L_PC = L_PB + 1280;
constexpr int L_VT = L_PC + 1280;
static_assert(L_VT % 4 == 0 && L_PART % 4 == 0 && L_VT - L_PART >= 5120, "alignment; room for the wq tile");

__device__ __forceinline__ float bflo(unsigned u) { return __uint_as_float(u << 16); }
__device__ __forceinline__ float bfhi(unsigned u) { return __uint_as_float(u & 0xffff0000u); }
__device__ __forceinline__ float rbf(float f) { float r = (float)(__bf16)f; asm("" : "+v"(r)); return r; }
__device__ __forceinline__ unsigned bfbits(float f) { return (unsigned)__builtin_bit_cast(u16, (__bf16)f); }
__device__ __forceinline__ float ex2(float x) { return __builtin_amdgcn_exp2f(x); }
__device__ __forceinline__ float silu(float x) { return x / (1.f + ex2(-x * LOG2E)); }
__device__ __forceinline__ float rcp(float x) { return __builtin_amdgcn_rcpf(x); }
__device__ __forceinline__ float silu_fast(float x) { return x * rcp(1.f + ex2(-x * LOG2E)); }
__device__ __forceinline__ float dot2(unsigned a, unsigned b, float acc) {
  return __builtin_amdgcn_fdot2_f32_bf16(__builtin_bit_cast(bf16x2, a), __builtin_bit_cast(bf16x2, b), acc, false);
}
template <int CTRL> __device__ __forceinline__ float dpp(float x) {
  return __builtin_bit_cast(float, __builtin_amdgcn_mov_dpp(__builtin_bit_cast(int, x), CTRL, 0xf, 0xf, true));
}
constexpr int XOR1 = 0xB1, XOR2 = 0x4E, HMIR = 0x141, MIR = 0x140, ROR4 = 0x124, ROR8 = 0x128, BCAST = 0x150;
__device__ __forceinline__ float tr32(float a, float b) {
  auto r = __builtin_amdgcn_permlane32_swap(__float_as_uint(a), __float_as_uint(b), false, false);
  return __uint_as_float(r[0]) + __uint_as_float(r[1]);
}
__device__ __forceinline__ float tr16(float a, float b) {
  auto r = __builtin_amdgcn_permlane16_swap(__float_as_uint(a), __float_as_uint(b), false, false);
  return __uint_as_float(r[0]) + __uint_as_float(r[1]);
}
__device__ __forceinline__ float max32(float a) {
  auto r = __builtin_amdgcn_permlane32_swap(__float_as_uint(a), __float_as_uint(a), false, false);
  return fmaxf(__uint_as_float(r[0]), __uint_as_float(r[1]));
}
__device__ __forceinline__ float max16(float a) {
  auto r = __builtin_amdgcn_permlane16_swap(__float_as_uint(a), __float_as_uint(a), false, false);
  return fmaxf(__uint_as_float(r[0]), __uint_as_float(r[1]));
}
template <int CTRL> __device__ __forceinline__ float trd(float a, float b, bool hi) {
  const float t1 = a + dpp<CTRL>(a), t2 = b + dpp<CTRL>(b);
  return hi ? t2 : t1;
}
__device__ __forceinline__ float sum_row16(float x) { x += dpp<XOR1>(x); x += dpp<XOR2>(x); x += dpp<HMIR>(x); x += dpp<MIR>(x); return x; }
__device__ __forceinline__ float max_row16(float x) { x = fmaxf(x, dpp<XOR1>(x)); x = fmaxf(x, dpp<XOR2>(x)); x = fmaxf(x, dpp<HMIR>(x)); return fmaxf(x, dpp<MIR>(x)); }
__device__ __forceinline__ float sum_lo8(float x) { x += dpp<XOR1>(x); x += dpp<XOR2>(x); x += dpp<HMIR>(x); return x; }
__device__ __forceinline__ float max_lo8(float x) { x = fmaxf(x, dpp<XOR1>(x)); x = fmaxf(x, dpp<XOR2>(x)); x = fmaxf(x, dpp<HMIR>(x)); return x; }
__device__ __forceinline__ float sum_hi8(float x) { x += dpp<ROR8>(x); x = tr16(x, x); x = tr32(x, x); return x; }
__device__ __forceinline__ float sum64(float x) { x = sum_row16(x); x = tr16(x, x); return tr32(x, x); }
__device__ __forceinline__ float max64(float x) { return max32(max16(max_row16(x))); }
__device__ __forceinline__ void unpack8(const v4u& w, float (&f)[8]) {
  f[0] = bflo(w.x); f[1] = bfhi(w.x); f[2] = bflo(w.y); f[3] = bfhi(w.y); f[4] = bflo(w.z); f[5] = bfhi(w.z); f[6] = bflo(w.w); f[7] = bfhi(w.w);
}

typedef __amdgpu_buffer_rsrc_t rsrc_t;
__device__ __forceinline__ rsrc_t mk(const void* p, unsigned bytes) { return __builtin_amdgcn_make_buffer_rsrc((void*)p, 0, (int)bytes, RSRC3); }
template <int POL> __device__ __forceinline__ v4u ld_p(rsrc_t r, int voff, int soff) { return __builtin_amdgcn_raw_buffer_load_b128(r, voff, soff, POL); }
#define ld_k ld_p<POL_K>
#define ld_v ld_p<POL_V>
#define ld_w ld_p<POL_W>
__device__ __forceinline__ v4u ld_g(rsrc_t r, int voff, int soff) { return __builtin_amdgcn_raw_buffer_load_b128(r, voff, soff, AUX_SC1); }
__device__ __forceinline__ void st_g(rsrc_t r, int voff, int soff, float a, float b, unsigned tA, unsigned tB) {
  v4u v = {__float_as_uint(a), tA, __float_as_uint(b), tB};
  __builtin_amdgcn_raw_buffer_store_b128(v, r, voff, soff, AUX_SC1);
}
__device__ __forceinline__ bool tag_ok(const v4u& g, unsigned tA, unsigned tB) { return g.y == tA && g.w == tB; }
template <int POL> __device__ __forceinline__ void glds(const void* gsrc, unsigned lds_dst) {
  unsigned keep;
  if (POL == 2)
    asm volatile("s_mov_b32 %0, m0\n\ts_mov_b32 m0, %2\n\ts_nop 0\n\tglobal_load_lds_dwordx4 %1, off nt\n\ts_mov_b32 m0, %0"
                 : "=&s"(keep) : "v"(gsrc), "s"(lds_dst) : "memory");
  else
    asm volatile("s_mov_b32 %0, m0\n\ts_mov_b32 m0, %2\n\ts_nop 0\n\tglobal_load_lds_dwordx4 %1, off\n\ts_mov_b32 m0, %0"
                 : "=&s"(keep) : "v"(gsrc), "s"(lds_dst) : "memory");
}
#define LDS_WAIT() asm volatile("s_waitcnt lgkmcnt(0)" ::: "memory")
#define VM_WAIT(n) asm volatile("s_waitcnt vmcnt(%0)" :: "i"(n) : "memory")
#define BAR() do { asm volatile("s_waitcnt lgkmcnt(0)" ::: "memory"); __builtin_amdgcn_s_barrier(); asm volatile("" ::: "memory"); } while (0)
#define SCHED() __builtin_amdgcn_sched_barrier(0)

template <int N, int SLP, typename OFF>
__device__ __forceinline__ unsigned poll(rsrc_t rs, int voff, OFF soff, bool active, unsigned tA, unsigned tB, v4u (&g)[N]) {
  unsigned spins = 0;
  for (; spins < SPIN_CAP; ++spins) {
    asm volatile("" ::: "memory");
    bool ok = true;
#pragma unroll
    for (int k = 0; k < N; ++k) g[k] = ld_g(rs, voff, soff(k));
#pragma unroll
    for (int k = 0; k < N; ++k) ok = ok && tag_ok(g[k], tA, tB);
    if (__all(ok || !active)) break;
    if (SLP) __builtin_amdgcn_s_sleep(SLP);
  }
  return spins;
}
template <int SLP>
__device__ __forceinline__ unsigned poll2(rsrc_t rs, int voff0, int soff0, int voff1, int soff1, unsigned tA, unsigned tB, v4u& g0, v4u& g1) {
  unsigned spins = 0;
  for (; spins < SPIN_CAP; ++spins) {
    asm volatile("" ::: "memory");
    g0 = ld_g(rs, voff0, soff0);
    g1 = ld_g(rs, voff1, soff1);
    if (__all(tag_ok(g0, tA, tB) && tag_ok(g1, tA, tB))) break;
    if (SLP) __builtin_amdgcn_s_sleep(SLP);
  }
  return spins;
}

__device__ __forceinline__ u64 rt() { return __builtin_amdgcn_s_memrealtime(); }
#define STAMPV(n, v) do { if (lane == (n)) tsv = (u64)(v) + 1; } while (0)
#define STAMP(n) do { const u64 t_ = rt(); if (lane == (n)) tsv = t_; } while (0)

template <int KWIN, int VP, int VL, int PSL, int D1, int D2, int D3, int D4, int TOUCH>
__global__ void __launch_bounds__(1024, 4)
adaln_decode_kernel(const u16* __restrict__ x, const u16* __restrict__ Kc, const u16* __restrict__ Vc, const u16* __restrict__ wq,
                    const u16* __restrict__ wo, const u16* __restrict__ wg, const u16* __restrict__ wu, const u16* __restrict__ wd,
                    const u16* __restrict__ cnd, const u16* __restrict__ wmod, u16* __restrict__ out, unsigned char* __restrict__ ws) {
  constexpr int VR = 16 - VP - VL;
  static_assert(VR >= 0 && VR <= 12, "");
  __shared__ __attribute__((aligned(16))) float lds[L_VT + 16 * (VL > 0 ? VL : 1) * 256];
  const int tid = threadIdx.x, lane = tid & 63, w = __builtin_amdgcn_readfirstlane(tid >> 6);
  const int b = blockIdx.x;
  const int h = (b & 7) * 4 + (b >> 6), s = (b >> 3) & 7;
  const int rho = lane >> 4, c16 = lane & 15;
  u64 tsv = 0;
  STAMP(0);
  const u64 clk0 = __builtin_amdgcn_s_memtime();
  volatile LAS unsigned* qflag = (volatile LAS unsigned*)&lds[L_FLAG];
  if (tid == 0) *qflag = 0u;
  const rsrc_t rws = mk(ws, WS_END);
  const size_t kvbase = ((size_t)h * SK + s * 1024) * 256;
  const rsrc_t rk = mk((const char*)Kc + kvbase, 262144);
  const rsrc_t rv = mk((const char*)Vc + kvbase, 262144);
  const int rot = ((b >> 3) * 5 + (b & 7) * 3) & 15;
  auto toff = [&](int i) { return ((((i + rot) & 15) << 4) + w) << 10; };
  const uint2 ep = *(const uint2*)(ws + WS_EPOCH);
  const unsigned tA = ep.x + 1u, tB = ep.y + 1u;
  BAR();

  v4u kv[16], vp[VP > 0 ? VP : 1];
  const unsigned vt_lds = (unsigned)(uintptr_t)(LAS float*)&lds[L_VT] + (unsigned)w * (VL * 1024);
  const char* vgl = (const char*)Vc + kvbase + lane * 16;
  if (w == 0) {
    const int cb = b % 60, rq = b / 60;
    const bool has_mod = b < 240;
    {
      const rsrc_t rm = mk((const char*)wmod + (size_t)(has_mod ? rq : 0) * 160 * 7680 + (has_mod ? cb : 0) * 128, has_mod ? 160 * 7680 : 0);
      v4u t[20];
      const int voff = (lane >> 3) * 7680 + (lane & 7) * 16;
      const int r0 = (has_mod ? rq : 0) * 160;
      u16 cv[3];
#pragma unroll
      for (int k = 0; k < 3; ++k) { const int i = lane + 64 * k; cv[k] = cnd[r0 + (i < 160 ? i : 0)]; }
#pragma unroll
      for (int u = 0; u < 20; ++u) t[u] = ld_w(rm, voff, u * 8 * 7680);
      SCHED();
#pragma unroll
      for (int k = 0; k < 3; ++k) lds[L_SC + lane + 64 * k] = rbf(silu(bflo(cv[k])));
      LDS_WAIT();
      float acc[8];
#pragma unroll
      for (int j = 0; j < 8; ++j) acc[j] = 0.f;
#pragma unroll
      for (int u = 0; u < 20; ++u) {
        const float sv = lds[L_SC + 8 * u + (lane >> 3)];
        float f[8]; unpack8(t[u], f);
#pragma unroll
        for (int j = 0; j < 8; ++j) acc[j] = fmaf(sv, f[j], acc[j]);
      }
      float y[4];
#pragma unroll
      for (int j = 0; j < 4; ++j) y[j] = tr32(acc[j], acc[j + 4]);
      float z0 = tr16(y[0], y[2]), z1 = tr16(y[1], y[3]);
      z0 += dpp<ROR8>(z0); z1 += dpp<ROR8>(z1);
      const int gi = 4 * (lane & 7) + rho;
      if ((lane & 8) == 0) *(LAS v2f*)&lds[L_TMP + 2 * gi] = v2f{z0, z1};
      LDS_WAIT();
      if (has_mod && lane < 32) { const v2f v = *(LAS v2f*)&lds[L_TMP + 2 * lane]; st_g(rws, lane * 16, WS_MOD + (rq * 1920 + cb * 32) * 16, v.x, v.y, tA, tB); }
    }
    STAMP(1);
    {
      const char* gq = (const char*)wq + (size_t)(s * 80 + rho) * 8192 + h * 256 + c16 * 16;
      const unsigned dst = (unsigned)(uintptr_t)(LAS float*)&lds[L_PART];
#pragma unroll
      for (int u = 0; u < 20; ++u) glds<POL_W>(gq + (size_t)u * 4 * 8192, dst + u * 1024);
    }
    SCHED();
    float mean_b, den_b;
    {
      float xv[10];
#pragma unroll
      for (int k = 0; k < 5; ++k) { const unsigned u = ((const unsigned*)x)[lane + 64 * k]; xv[2 * k] = bflo(u); xv[2 * k + 1] = bfhi(u); }
      float sm = 0.f;
#pragma unroll
      for (int k = 0; k < 10; ++k) sm += xv[k];
      const float mean = sum64(sm) * (1.f / D);
      float vs = 0.f;
#pragma unroll
      for (int k = 0; k < 10; ++k) { const float d = xv[k] - mean; vs = fmaf(d, d, vs); }
      const float var = sum64(vs) * (1.f / D);
      mean_b = rbf(mean); den_b = rbf(sqrtf(rbf(rbf(var) + 1e-5f)));
    }
    const int j40 = lane < 40 ? lane : 0;
    const unsigned xs2 = ((const unsigned*)x)[40 * s + j40];
    if (lane < 40) *(LAS v2f*)&lds[L_XS + 2 * lane] = v2f{bflo(xs2), bfhi(xs2)};
    {
      v4u g[8];
      poll<8, PSL>(rws, j40 * 16, [&](int k) { return WS_MOD + ((k & 3) * 1920 + (k >> 2) * 320 + 40 * s) * 16; }, lane < 40, tA, tB, g);
      const float sa0 = rbf((__uint_as_float(g[0].x) + __uint_as_float(g[1].x)) + (__uint_as_float(g[2].x) + __uint_as_float(g[3].x)));
      const float sa1 = rbf((__uint_as_float(g[0].z) + __uint_as_float(g[1].z)) + (__uint_as_float(g[2].z) + __uint_as_float(g[3].z)));
      const float ca0 = rbf((__uint_as_float(g[4].x) + __uint_as_float(g[5].x)) + (__uint_as_float(g[6].x) + __uint_as_float(g[7].x)));
      const float ca1 = rbf((__uint_as_float(g[4].z) + __uint_as_float(g[5].z)) + (__uint_as_float(g[6].z) + __uint_as_float(g[7].z)));
      const float h0 = rbf(rbf(rbf(rbf(bflo(xs2) - mean_b) / den_b) * rbf(1.f + ca0)) + sa0);
      const float h1 = rbf(rbf(rbf(rbf(bfhi(xs2) - mean_b) / den_b) * rbf(1.f + ca1)) + sa1);
      if (lane < 40) *(LAS v2f*)&lds[L_HS + 2 * lane] = v2f{h0, h1};
      VM_WAIT(0);
      LDS_WAIT();
    }
    STAMP(2);
    {
      float acc[8];
#pragma unroll
      for (int j = 0; j < 8; ++j) acc[j] = 0.f;
#pragma unroll
      for (int u = 0; u < 20; ++u) {
        const float hv = lds[L_HS + 4 * u + rho];
        const v4u tq = *(LAS v4u*)&lds[L_PART + u * 256 + lane * 4];
        float f[8]; unpack8(tq, f);
#pragma unroll
        for (int j = 0; j < 8; ++j) acc[j] = fmaf(hv, f[j], acc[j]);
      }
      float y[4];
#pragma unroll
      for (int j = 0; j < 4; ++j) y[j] = tr32(acc[j], acc[j + 4]);
      const float z0 = tr16(y[0], y[2]), z1 = tr16(y[1], y[3]);
      st_g(rws, lane * 16, WS_Q + (h * 8 + s) * 1024, z0, z1, tA, tB);
    }
    STAMP(3);
    {
      v4u g[8];
      poll<8, 0>(rws, lane * 16, [&](int k) { return WS_Q + (h * 8 + k) * 1024; }, true, tA, tB, g);
      float q0 = 0.f, q1 = 0.f;
#pragma unroll
      for (int k = 0; k < 8; ++k) { q0 += __uint_as_float(g[k].x); q1 += __uint_as_float(g[k].z); }
      ((LAS unsigned*)lds)[L_Q + 4 * c16 + rho] = bfbits(q0) | (bfbits(q1) << 16);
    }
    LDS_WAIT();
    *qflag = 1u;
    STAMP(4);
#pragma unroll
    for (int i = 0; i < 16; ++i) kv[i] = ld_k(rk, lane * 16, toff(i));
    SCHED();
#pragma unroll
    for (int i = 0; i < VP; ++i) vp[i] = ld_v(rv, lane * 16, toff(i));
#pragma unroll
    for (int i = 0; i < VL; ++i) glds<POL_V>(vgl + toff(VP + i), vt_lds + i * 1024);
    SCHED();
#pragma unroll
    for (int ch = 2; ch < 6; ++ch) {
      v4u g[4];
      poll<4, 1>(rws, j40 * 16, [&](int k) { return WS_MOD + (k * 1920 + ch * 320 + 40 * s) * 16; }, lane < 40, tA, tB, g);
      const float m0 = rbf((__uint_as_float(g[0].x) + __uint_as_float(g[1].x)) + (__uint_as_float(g[2].x) + __uint_as_float(g[3].x)));
      const float m1 = rbf((__uint_as_float(g[0].z) + __uint_as_float(g[1].z)) + (__uint_as_float(g[2].z) + __uint_as_float(g[3].z)));
      if (lane < 40) *(LAS v2f*)&lds[L_MOD + ch * 80 + 2 * lane] = v2f{m0, m1};
    }
    VM_WAIT(0);
  } else {
    if (TOUCH && w == 15) {
      const int g_ = h < 27 ? h : 0;
      int o0;
      if (lane < 9) o0 = WS_O + (h * 8 + s) * 1280 + lane * 128;
      else if (lane < 19) o0 = WS_Y + (s * 10 * 32 + h) * 64 + (lane - 9) * 2048;
      else if (lane < 27) o0 = WS_A + (g_ * 512 + s * 8) * 16 + (lane - 19) * 1024;
      else if (lane < 35) o0 = WS_B + (g_ * 512 + s * 8) * 16 + (lane - 27) * 1024;
      else if (lane < 43) o0 = WS_C + (g_ * 512 + s * 8) * 16 + (lane - 35) * 1024;
      else o0 = WS_S + (g_ * 8 + s) * 128;
      const unsigned t0_ = __builtin_amdgcn_raw_buffer_load_b32(rws, o0, 0, TOUCH == 1 ? AUX_SC1 : 0);
      const unsigned t1_ = __builtin_amdgcn_raw_buffer_load_b32(rws, (lane < 40 ? lane : 0) * 128, WS_D + (g_ * 8 + s) * 5120, TOUCH == 1 ? AUX_SC1 : 0);
      asm volatile("" :: "v"(t0_), "v"(t1_));
    }
#pragma unroll
    for (int i = 0; i < 16; ++i) {
      kv[i] = ld_k(rk, lane * 16, toff(i));
      if (i >= KWIN - 1) VM_WAIT(KWIN - 1);
      SCHED();
    }
#pragma unroll
    for (int i = 0; i < VP; ++i) { vp[i] = ld_v(rv, lane * 16, toff(i)); VM_WAIT(KWIN - 1); SCHED(); }
#pragma unroll
    for (int i = 0; i < VL; ++i) { glds<POL_V>(vgl + toff(VP + i), vt_lds + i * 1024); if (i < VL - 1) VM_WAIT(KWIN - 1); SCHED(); }
    STAMP(4);
    for (unsigned spins = 0; spins < (SPIN_CAP << 6); ++spins) { if (*qflag != 0u) break; __builtin_amdgcn_s_sleep(1); }
    asm volatile("" ::: "memory");
  }
  STAMP(5);
  const bool ffn = h < 27;
  const bool gw = ffn && w < 10, dw = ffn && w >= 10 && w < 15;
  const rsrc_t ro = mk((const char*)wo + (size_t)(h * 128 + 8 * w) * 1280 + s * 160, 8 * 1280);
  const int voff_o = c16 < 10 ? rho * 1280 + c16 * 16 : 0x7fffff00;
  const size_t goff = (size_t)(80 * s + 8 * (gw ? w : 0)) * 3456 + (gw ? h : 0) * 128;
  const rsrc_t rf0 = gw ? mk((const char*)wg + goff, 8 * 3456) : mk((const char*)wd + (size_t)(dw ? 64 * h + 8 * s : 0) * 1280, dw ? 8 * 1280 : 0);
  const rsrc_t rf1 = gw ? mk((const char*)wu + goff, 8 * 3456) : mk((const char*)wd + (size_t)(dw ? 64 * h + 8 * s : 0) * 1280 + 1280, dw ? 7 * 1280 : 0);
  const int voff_f = gw ? (lane >> 3) * 3456 + (lane & 7) * 16 : (lane & 3) * 2560 + ((dw ? w - 10 : 0) * 16 + (lane >> 2)) * 16;
  v4u wot[2], fwt[2];
  const v4u qf = *(LAS v4u*)&lds[L_Q + 4 * c16];
  float sc;
  {
    float xk[16];
#pragma unroll
    for (int i = 0; i < 16; ++i) {
      float d = dot2(kv[i].x, qf.x, 0.f); d = dot2(kv[i].y, qf.y, d); d = dot2(kv[i].z, qf.z, d); d = dot2(kv[i].w, qf.w, d);
      xk[i] = d;
      asm volatile("" : "+v"(xk[i]));
      if (i == 0) wot[0] = ld_w(ro, voff_o, 0);
      if (i == 1) wot[1] = ld_w(ro, voff_o, 4 * 1280);
      SCHED();
    }
#pragma unroll
    for (int j = 0; j < 8; ++j) xk[j] = trd<ROR8>(xk[j], xk[j + 8], (lane & 8) != 0);
#pragma unroll
    for (int j = 0; j < 4; ++j) xk[j] = trd<HMIR>(xk[j], xk[j + 4], (lane & 4) != 0);
#pragma unroll
    for (int j = 0; j < 2; ++j) xk[j] = trd<XOR2>(xk[j], xk[j + 2], (lane & 2) != 0);
    const float dot = trd<XOR1>(xk[0], xk[1], (lane & 1) != 0);
    sc = rbf(rbf(dot) * 0.08838834764831845f);
  }
  const float mw = max64(sc);
  const float p = ex2((sc - mw) * LOG2E);
  { const float lw = sum64(p); if (lane == 0) { lds[L_WM + w] = mw; lds[L_WL + w] = lw; } }
  STAMP(6);
  float acc[8];
#pragma unroll
  for (int j = 0; j < 8; ++j) acc[j] = 0.f;
#define PV_ACC(i, reg) { const float pb = dpp<BCAST + (i)>(p); float f[8]; unpack8(reg, f); \
    _Pragma("unroll") for (int j = 0; j < 8; ++j) acc[j] = fmaf(pb, f[j], acc[j]); }
  VM_WAIT(1); fwt[0] = ld_w(rf0, voff_f, 0); SCHED();
  VM_WAIT(1); fwt[1] = ld_w(rf1, voff_f, 0); SCHED();
#define PV_LANDED(i) if ((i) < VP) PV_ACC(i, vp[(i) < VP ? (i) : 0]) \
    else if ((i) < VP + VL) { const v4u t_ = *(LAS v4u*)&lds[L_VT + (w * VL + ((i) - VP)) * 256 + lane * 4]; PV_ACC(i, t_) }
  PV_LANDED(0) PV_LANDED(1) PV_LANDED(2) PV_LANDED(3) PV_LANDED(4) PV_LANDED(5) PV_LANDED(6) PV_LANDED(7)
  PV_LANDED(8) PV_LANDED(9) PV_LANDED(10) PV_LANDED(11) PV_LANDED(12) PV_LANDED(13) PV_LANDED(14) PV_LANDED(15)
#undef PV_LANDED
  SCHED();
#pragma unroll
  for (int i = 0; i < VR; ++i) { VM_WAIT(KWIN - 1); kv[i] = ld_v(rv, lane * 16, toff(VP + VL + i)); SCHED(); }
#define PV_LATE(i) if ((i) >= VP + VL) PV_ACC(i, kv[(i) >= VP + VL ? (i) - VP - VL : 0])
  PV_LATE(0) PV_LATE(1) PV_LATE(2) PV_LATE(3) PV_LATE(4) PV_LATE(5) PV_LATE(6) PV_LATE(7)
  PV_LATE(8) PV_LATE(9) PV_LATE(10) PV_LATE(11) PV_LATE(12) PV_LATE(13) PV_LATE(14) PV_LATE(15)
#undef PV_LATE
#undef PV_ACC
  STAMP(7);
  {
    float y[4];
#pragma unroll
    for (int j = 0; j < 4; ++j) y[j] = tr32(acc[j], acc[j + 4]);
    const float z0 = tr16(y[0], y[2]), z1 = tr16(y[1], y[3]);
    *(LAS v2f*)&lds[L_PART + w * 128 + 8 * c16 + 2 * rho] = v2f{z0, z1};
  }
  BAR();
  STAMP(8);
  const int r8 = lane >> 3;
  float gf[8], uf[8];
  float bmul = 0.f;
  if (w == 15) {
    __builtin_amdgcn_s_setprio(3);
    const int hi = lane >> 5;
    const float wm_a = lds[L_WM + c16], wm_b = lds[L_WM + (c16 & 7) + 8 * hi], wl_a = lds[L_WL + c16];
    v4f pv[8];
#pragma unroll
    for (int k = 0; k < 8; ++k) pv[k] = *(LAS v4f*)&lds[L_PART + (k + 8 * hi) * 128 + 4 * (lane & 31)];
    SCHED();
    const float Mcu = max_row16(wm_a);
    const float fk = ex2((wm_b - Mcu) * LOG2E);
    const float Lcu = sum_row16(ex2((wm_a - Mcu) * LOG2E) * wl_a);
    v4f o4 = {0.f, 0.f, 0.f, 0.f};
#define MRG(k) { const float f = dpp<BCAST + (k)>(fk); o4 += f * pv[k]; }
    MRG(0) MRG(1) MRG(2) MRG(3) MRG(4) MRG(5) MRG(6) MRG(7)
#undef MRG
    st_g(rws, lane * 16, WS_O + (h * 8 + s) * 1280, tr32(o4.x, o4.z), tr32(o4.y, o4.w), tA, tB);
    if (lane == 0) st_g(rws, 64 * 16, WS_O + (h * 8 + s) * 1280, Mcu, Lcu, tA, tB);
    STAMP(9);
    if (D1) __builtin_amdgcn_s_sleep(D1);
    v4u go[8], gml;
    for (unsigned spins = 0; spins < SPIN_CAP; ++spins) {
      asm volatile("" ::: "memory");
#pragma unroll
      for (int k = 0; k < 8; ++k) go[k] = ld_g(rws, lane * 16, WS_O + (h * 8 + k) * 1280);
      gml = ld_g(rws, (lane & 7) * 1280 + 64 * 16, WS_O + h * 8 * 1280);
      unsigned bad = (gml.y ^ tA) | (gml.w ^ tB);
#pragma unroll
      for (int k = 0; k < 8; ++k) bad |= (go[k].y ^ tA) | (go[k].w ^ tB);
      if (__all(bad == 0u)) break;
      __builtin_amdgcn_s_sleep(2);
    }
    STAMP(10);
    const float m = __uint_as_float(gml.x), l = __uint_as_float(gml.z);
    const float M = max_lo8(m);
    const float f = ex2((m - M) * LOG2E);
    const float rL = rcp(sum_lo8(f * l));
    float o0 = 0.f, o1 = 0.f;
#define CMB(k) { const float fb = __uint_as_float(__builtin_amdgcn_readlane(__float_as_uint(f), k)); o0 = fmaf(fb, __uint_as_float(go[k].x), o0); o1 = fmaf(fb, __uint_as_float(go[k].z), o1); }
    CMB(0) CMB(1) CMB(2) CMB(3) CMB(4) CMB(5) CMB(6) CMB(7)
#undef CMB
    *(LAS v2f*)&lds[L_OS + 4 * (lane & 31) + 2 * hi] = v2f{rbf(o0 * rL), rbf(o1 * rL)};
  } else if (gw) {
    unpack8(fwt[0], gf); unpack8(fwt[1], uf);
    bmul = rbf(1.f + lds[L_MOD + 4 * 80 + 8 * w + r8]);
    const float cmul = lds[L_MOD + 3 * 80 + 8 * w + r8];
#pragma unroll
    for (int t = 0; t < 2; ++t) {
      const float mul = t == 0 ? bmul : cmul;
      float y[8];
#pragma unroll
      for (int j = 0; j < 8; ++j) y[j] = tr32(mul * gf[j], mul * uf[j]);
      float z[4];
#pragma unroll
      for (int j = 0; j < 4; ++j) z[j] = tr16(y[j], y[j + 4]);
      const float e0 = trd<ROR8>(z[0], z[2], (lane & 8) != 0), e1 = trd<ROR8>(z[1], z[3], (lane & 8) != 0);
      *(LAS v2f*)&lds[(t == 0 ? L_PB : L_PC) + w * 128 + (lane >> 5) * 64 + 8 * (lane & 7) + 4 * ((lane >> 4) & 1) + 2 * ((lane >> 3) & 1)] = v2f{e0, e1};
    }
  }
  BAR();
  STAMP(11);
  {
    const float o0 = lds[L_OS + 8 * w + rho], o1 = lds[L_OS + 8 * w + 4 + rho];
    float f0[8], f1[8]; unpack8(wot[0], f0); unpack8(wot[1], f1);
    float a[8];
#pragma unroll
    for (int j = 0; j < 8; ++j) a[j] = fmaf(o0, f0[j], o1 * f1[j]);
    float y[4];
#pragma unroll
    for (int j = 0; j < 4; ++j) y[j] = tr32(a[j], a[j + 4]);
    const float z0 = tr16(y[0], y[2]), z1 = tr16(y[1], y[3]);
    if (c16 < 10) *(LAS v2f*)&lds[L_PY + w * 80 + 8 * c16 + 2 * rho] = v2f{z0, z1};
  }
  STAMP(12);
  BAR();
  STAMP(13);
  const int j40 = lane < 40 ? lane : 0;
  if (w == 15) {
    v2f pv[16];
#pragma unroll
    for (int k = 0; k < 16; ++k) pv[k] = *(LAS v2f*)&lds[L_PY + k * 80 + 2 * j40];
    SCHED();
    v2f ya = (pv[0] + pv[1]) + (pv[2] + pv[3]), yb = (pv[4] + pv[5]) + (pv[6] + pv[7]), yc = (pv[8] + pv[9]) + (pv[10] + pv[11]), yd = (pv[12] + pv[13]) + (pv[14] + pv[15]);
    ya = (ya + yb) + (yc + yd);
    const float y0 = ya.x, y1 = ya.y;
    if (lane < 40) st_g(rws, ((lane >> 2) * 32 * 4 + (lane & 3)) * 16, WS_Y + (s * 10 * 32 + h) * 64, y0, y1, tA, tB);
    STAMP(14);
  }
  if (ffn) {
    const int g = h, r = s;
    const int sig = ((lane & 7) >> 1) | ((lane & 1) << 2);
    if (w == 10 || w == 11) {
      const int pb = w == 10 ? L_PB : L_PC;
      float a0 = 0.f, a1 = 0.f;
#pragma unroll
      for (int k = 0; k < 10; ++k) { a0 += lds[pb + k * 128 + lane]; a1 += lds[pb + k * 128 + 64 + lane]; }
      st_g(rws, ((lane >> 3) * 64 + sig) * 16, (w == 10 ? WS_B : WS_C) + (g * 512 + r * 8) * 16, a0, a1, tA, tB);
    }
    float Bg = 0.f, Bu = 0.f, Cg = 0.f, Cu = 0.f;
    if (w == 10) {
      __builtin_amdgcn_s_sleep(16);
      v4u gb[2];
      poll<2, 8>(rws, lane * 16, [&](int k) { return (k == 0 ? WS_B : WS_C) + (g * 8 + r) * 1024; }, true, tA, tB, gb);
      Bg = sum_hi8(__uint_as_float(gb[0].x)); Bu = sum_hi8(__uint_as_float(gb[0].z));
      Cg = sum_hi8(__uint_as_float(gb[1].x)); Cu = sum_hi8(__uint_as_float(gb[1].z));
    }
    if (w < 10) {
      if (D2) __builtin_amdgcn_s_sleep(D2);
      v4u gy[2];
      const unsigned np2 = poll<2, PSL>(rws, c16 * 64 + rho * 16, [&](int k) { return WS_Y + ((r * 10 + w) * 32 + 16 * k) * 64; }, true, tA, tB, gy);
      STAMPV(26, np2);
      STAMP(16);
      const float y0 = sum_row16(__uint_as_float(gy[0].x) + __uint_as_float(gy[1].x));
      const float y1 = sum_row16(__uint_as_float(gy[0].z) + __uint_as_float(gy[1].z));
      const float attn = rbf((lane & 8) ? y1 : y0);
      const int cl = 8 * w + r8;
      const float x1 = rbf(lds[L_XS + cl] + rbf(lds[L_MOD + 2 * 80 + cl] * attn));
      const float xs = x1 * bmul;
      float y[8];
#pragma unroll
      for (int j = 0; j < 8; ++j) y[j] = tr32(xs * gf[j], xs * uf[j]);
      float z[4];
#pragma unroll
      for (int j = 0; j < 4; ++j) z[j] = tr16(y[j], y[j + 4]);
      const float e0 = trd<ROR8>(z[0], z[2], (lane & 8) != 0), e1 = trd<ROR8>(z[1], z[3], (lane & 8) != 0);
      *(LAS v2f*)&lds[L_PA + w * 128 + (lane >> 5) * 64 + 8 * (lane & 7) + 4 * ((lane >> 4) & 1) + 2 * ((lane >> 3) & 1)] = v2f{e0, e1};
      const float mean_w = sum_hi8(x1) * 0.125f;
      const float dd = x1 - mean_w;
      const float m2_w = sum_hi8(dd * dd);
      if (lane == 0) *(LAS v2f*)&lds[L_STAT + 2 * w] = v2f{mean_w, m2_w};
      if ((lane & 7) == 0) lds[L_X1 + cl] = x1;
    }
    STAMP(17);
    BAR();
    STAMP(18);
    if (w == 0) {
      float pa[10], pb_[10];
#pragma unroll
      for (int k = 0; k < 10; ++k) { pa[k] = lds[L_PA + k * 128 + lane]; pb_[k] = lds[L_PA + k * 128 + 64 + lane]; }
      SCHED();
      const float a0 = ((pa[0] + pa[1]) + (pa[2] + pa[3])) + ((pa[4] + pa[5]) + (pa[6] + pa[7])) + (pa[8] + pa[9]);
      const float a1 = ((pb_[0] + pb_[1]) + (pb_[2] + pb_[3])) + ((pb_[4] + pb_[5]) + (pb_[6] + pb_[7])) + (pb_[8] + pb_[9]);
      st_g(rws, ((lane >> 3) * 64 + sig) * 16, WS_A + (g * 512 + r * 8) * 16, a0, a1, tA, tB);
      STAMP(19);
    } else if (w == 1) {
      const v2f sv = *(LAS v2f*)&lds[L_STAT + 2 * (c16 < 10 ? c16 : 0)];
      const float mk_ = c16 < 10 ? sv.x : 0.f;
      const float mean_r = sum_row16(mk_) * 0.1f;
      const float dd = sv.x - mean_r;
      const float m2 = sum_row16(c16 < 10 ? fmaf(8.f * dd, dd, sv.y) : 0.f);
      if (lane == 0) st_g(rws, 0, WS_S + (g * 8 + r) * 128, mean_r, m2, tA, tB);
      STAMP(15);
    } else if (w == 10) {
      if (D3) __builtin_amdgcn_s_sleep(D3);
      v4u ga, gs;
      STAMP(25);
      const unsigned np3 = poll2<1>(rws, lane * 16, WS_A + (g * 8 + r) * 1024, (lane >> 3) * 128, WS_S + g * 1024, tA, tB, ga, gs);
      STAMPV(27, np3);
      STAMP(20);
      const float Ag = sum_hi8(__uint_as_float(ga.x)), Au = sum_hi8(__uint_as_float(ga.z));
      const float mr = __uint_as_float(gs.x);
      const float mean = sum_hi8(mr) * 0.125f;
      const float dd = mr - mean;
      const float var = sum_hi8(fmaf(80.f * dd, dd, __uint_as_float(gs.z))) * (1.f / D);
      const float mb = rbf(mean), den = rbf(__builtin_amdgcn_sqrtf(rbf(rbf(var) + 1e-5f)));
      const float rden = rcp(den);
      const float gate = rbf(fmaf(Ag - mb * Bg, rden, Cg)), up = rbf(fmaf(Au - mb * Bu, rden, Cu));
      const float act = rbf(rbf(silu_fast(gate)) * up);
      if (lane < 8) lds[L_ACT + 2 * (lane & 3) + (lane >> 2)] = act;
    }
    BAR();
    STAMP(21);
    if (dw) {
      const v2f av = *(LAS v2f*)&lds[L_ACT + 2 * (lane & 3)];
      float f0[8], f1[8]; unpack8(fwt[0], f0); unpack8(fwt[1], f1);
      float d[8];
#pragma unroll
      for (int j = 0; j < 8; ++j) d[j] = fmaf(av.x, f0[j], av.y * f1[j]);
      float y[4];
#pragma unroll
      for (int j = 0; j < 4; ++j) y[j] = trd<XOR2>(d[j], d[j + 4], (lane & 2) != 0);
      const float e0 = trd<XOR1>(y[0], y[2], (lane & 1) != 0), e1 = trd<XOR1>(y[1], y[3], (lane & 1) != 0);
      st_g(rws, lane * 16, WS_D + ((g * 8 + r) * 320 + (w - 10) * 64) * 16, e0, e1, tA, tB);
      STAMP(22);
    }
    if (g < 5) {
      if (D4) __builtin_amdgcn_s_sleep(D4);
      v4u gd[2];
      const int n1 = w + 16 < 27 ? w + 16 : w;
      const int voff = (lane >> 3) * 5120 + (lane & 7) * 16;
      STAMP(23);
      const unsigned np4 = poll<2, 1>(rws, voff, [&](int k) { return WS_D + (k == 0 ? w : n1) * 8 * 5120 + (40 * r + 8 * g) * 16; }, true, tA, tB, gd);
      STAMPV(28, np4);
      STAMP(24);
      float e0 = __uint_as_float(gd[0].x), e1 = __uint_as_float(gd[0].z);
      if (w + 16 < 27) { e0 += __uint_as_float(gd[1].x); e1 += __uint_as_float(gd[1].z); }
      e0 = sum_hi8(e0); e1 = sum_hi8(e1);
      if (lane < 8) *(LAS v2f*)&lds[L_PD + w * 16 + 2 * lane] = v2f{e0, e1};
      BAR();
      if (w == 0 && lane < 8) {
        v2f pv[16];
#pragma unroll
        for (int k = 0; k < 16; ++k) pv[k] = *(LAS v2f*)&lds[L_PD + k * 16 + 2 * lane];
        const int cl = 16 * g + 2 * lane;
        const v2f x1v = *(LAS v2f*)&lds[L_X1 + cl], gbv = *(LAS v2f*)&lds[L_MOD + 5 * 80 + cl];
        SCHED();
        v2f ya = (pv[0] + pv[1]) + (pv[2] + pv[3]), yb = (pv[4] + pv[5]) + (pv[6] + pv[7]), yc = (pv[8] + pv[9]) + (pv[10] + pv[11]), yd = (pv[12] + pv[13]) + (pv[14] + pv[15]);
        ya = (ya + yb) + (yc + yd);
        const float f0 = ya.x, f1 = ya.y;
        const float o0 = x1v.x + rbf(gbv.x * rbf(f0));
        const float o1 = x1v.y + rbf(gbv.y * rbf(f1));
        ((unsigned*)out)[(80 * r + cl) >> 1] = bfbits(o0) | (bfbits(o1) << 16);
        if (b == 0 && lane == 0) *(uint2*)(ws + WS_EPOCH) = make_uint2(tA, tB);
      }
    }
  }
  STAMP(30);
  { const u64 dc = __builtin_amdgcn_s_memtime() - clk0; if (lane == 31) tsv = dc; }
  if (lane < 32) ((u64*)(ws + WS_DBG))[(size_t)(b * 16 + w) * 32 + lane] = tsv;
}

template <int KWIN, int VP, int VL, int PSL, int D1, int D2, int D3, int D4, int TOUCH>
static void launch(void* const* d_in, void* d_out, void* d_ws, hipStream_t stream) {
  adaln_decode_kernel<KWIN, VP, VL, PSL, D1, D2, D3, D4, TOUCH><<<256, 1024, 0, stream>>>(
      (const u16*)d_in[0], (const u16*)d_in[1], (const u16*)d_in[2], (const u16*)d_in[3], (const u16*)d_in[4], (const u16*)d_in[6],
      (const u16*)d_in[7], (const u16*)d_in[8], (const u16*)d_in[9], (const u16*)d_in[10], (u16*)d_out, (unsigned char*)d_ws);
}

__global__ void k_wait(const unsigned char* ws) {
  __shared__ unsigned long long mn, mx;
  const u64* hs = (const u64*)(ws + WS_DBG);
  if (threadIdx.x == 0) { mn = ~0ull; mx = 0; }
  __syncthreads();
  for (int i = threadIdx.x; i < 4096; i += blockDim.x) atomicMin(&mn, hs[i * 32]);
  __syncthreads();
  for (int i = threadIdx.x; i < 4096; i += blockDim.x) { u64 v = hs[i * 32 + 0]; if (v > mn && v - mn < 100000) atomicMax(&mx, v - mn); }
  __syncthreads();
  if (threadIdx.x == 0) { const u64 t0 = rt(); const u64 d = mx * 30; while (rt() - t0 < d) __builtin_amdgcn_s_sleep(8); }
}

}

extern "C" void kernel_launch(void* const* d_in, const int* in_sizes, int n_in, void* d_out, int out_size, void* d_ws, size_t ws_size,
                              hipStream_t stream) {
  adaln::launch<2, 6, 6, 8, 8, 8, 8, 8, 2>(d_in, d_out, d_ws, stream);
  adaln::k_wait<<<1, 1024, 0, stream>>>((const unsigned char*)d_ws);
}
```

```cpp
#include <hip/hip_runtime.h>
#include <stdint.h>

#define DBG_STAMPS 0
#define POL_K 2
#define POL_V 2
#define POL_W 2

namespace adaln {

typedef unsigned short u16;
typedef unsigned long long u64;
typedef unsigned v4u __attribute__((ext_vector_type(4)));
typedef float v2f __attribute__((ext_vector_type(2)));
typedef float v4f __attribute__((ext_vector_type(4)));
typedef __bf16 bf16x2 __attribute__((ext_vector_type(2)));
#define LAS __attribute__((address_space(3)))

constexpr int D = 640, SK = 8192, F = 1728;
constexpr int AUX_NT = 2, AUX_SC1 = 16;
constexpr int RSRC3 = 0x00020000;
constexpr float LOG2E = 1.4426950408889634f;
constexpr unsigned SPIN_CAP = 1u << 16;

constexpr int WS_EPOCH = 0;
constexpr int WS_MOD = 256;
constexpr int WS_Q = WS_MOD + 4 * 1920 * 16;
constexpr int WS_O = WS_Q + 32 * 8 * 64 * 16;
constexpr int WS_Y = WS_O + 32 * 8 * 80 * 16;
constexpr int WS_A = WS_Y + 8 * 10 * 32 * 4 * 16;
constexpr int WS_B = WS_A + 27 * 8 * 64 * 16;
constexpr int WS_C = WS_B + 27 * 8 * 64 * 16;
constexpr int WS_S = WS_C + 27 * 8 * 64 * 16;
constexpr int WS_D = WS_S + 27 * 8 * 128;
constexpr int WS_END = WS_D + 216 * 320 * 16;
constexpr int WS_DBG = 4 << 20;

constexpr int L_Q = 0;
constexpr int L_FLAG = L_Q + 64;
constexpr int L_HS = L_FLAG + 4;
constexpr int L_SC = L_HS + 80;
constexpr int L_TMP = L_SC + 192;
constexpr int L_MOD = L_TMP + 64;
constexpr int L_WM = L_MOD + 480;
constexpr int L_WL = L_WM + 16;
constexpr int L_STAT = L_WL + 16;
constexpr int L_X1 = L_STAT + 32;
constexpr int L_XS = L_X1 + 80;
constexpr int L_OS = L_XS + 80;
constexpr int L_ACT = L_OS + 128;
constexpr int L_PD = L_ACT + 16;
constexpr int L_PART = L_PD + 256;
constexpr int L_PY = L_PART + 2048;
constexpr int L_PA = L_PY + 1280;
constexpr int L_PB = L_PA + 1280;
constexpr int L_PC = L_PB + 1280;
constexpr int L_VT = L_PC + 1280;
static_assert(L_VT % 4 == 0 && L_PART % 4 == 0 && L_VT - L_PART >= 5120, "alignment; room for the wq tile");

__device__ __forceinline__ float bflo(unsigned u) { return __uint_as_float(u << 16); }
__device__ __forceinline__ float bfhi(unsigned u) { return __uint_as_float(u & 0xffff0000u); }
__device__ __forceinline__ float rbf(float f) { float r = (float)(__bf16)f; asm("" : "+v"(r)); return r; }
__device__ __forceinline__ unsigned bfbits(float f) { return (unsigned)__builtin_bit_cast(u16, (__bf16)f); }
__device__ __forceinline__ float ex2(float x) { return __builtin_amdgcn_exp2f(x); }
__device__ __forceinline__ float silu(float x) { return x / (1.f + ex2(-x * LOG2E)); }
__device__ __forceinline__ float rcp(float x) { return __builtin_amdgcn_rcpf(x); }
__device__ __forceinline__ float silu_fast(float x) { return x * rcp(1.f + ex2(-x * LOG2E)); }
__device__ __forceinline__ float dot2(unsigned a, unsigned b, float acc) {
  return __builtin_amdgcn_fdot2_f32_bf16(__builtin_bit_cast(bf16x2, a), __builtin_bit_cast(bf16x2, b), acc, false);
}
template <int CTRL> __device__ __forceinline__ float dpp(float x) {
  return __builtin_bit_cast(float, __builtin_amdgcn_mov_dpp(__builtin_bit_cast(int, x), CTRL, 0xf, 0xf, true));
}
constexpr int XOR1 = 0xB1, XOR2 = 0x4E, HMIR = 0x141, MIR = 0x140, ROR4 = 0x124, ROR8 = 0x128, BCAST = 0x150;
__device__ __forceinline__ float tr32(float a, float b) {
  auto r = __builtin_amdgcn_permlane32_swap(__float_as_uint(a), __float_as_uint(b), false, false);
  return __uint_as_float(r[0]) + __uint_as_float(r[1]);
}
__device__ __forceinline__ float tr16(float a, float b) {
  auto r = __builtin_amdgcn_permlane16_swap(__float_as_uint(a), __float_as_uint(b), false, false);
  return __uint_as_float(r[0]) + __uint_as_float(r[1]);
}
__device__ __forceinline__ float max32(float a) {
  auto r = __builtin_amdgcn_permlane32_swap(__float_as_uint(a), __float_as_uint(a), false, false);
  return fmaxf(__uint_as_float(r[0]), __uint_as_float(r[1]));
}
__device__ __forceinline__ float max16(float a) {
  auto r = __builtin_amdgcn_permlane16_swap(__float_as_uint(a), __float_as_uint(a), false, false);
  return fmaxf(__uint_as_float(r[0]), __uint_as_float(r[1]));
}
template <int CTRL> __device__ __forceinline__ float trd(float a, float b, bool hi) {
  const float t1 = a + dpp<CTRL>(a), t2 = b + dpp<CTRL>(b);
  return hi ? t2 : t1;
}
__device__ __forceinline__ float sum_row16(float x) { x += dpp<XOR1>(x); x += dpp<XOR2>(x); x += dpp<HMIR>(x); x += dpp<MIR>(x); return x; }
__device__ __forceinline__ float max_row16(float x) { x = fmaxf(x, dpp<XOR1>(x)); x = fmaxf(x, dpp<XOR2>(x)); x = fmaxf(x, dpp<HMIR>(x)); return fmaxf(x, dpp<MIR>(x)); }
__device__ __forceinline__ float sum_lo8(float x) { x += dpp<XOR1>(x); x += dpp<XOR2>(x); x += dpp<HMIR>(x); return x; }
__device__ __forceinline__ float max_lo8(float x) { x = fmaxf(x, dpp<XOR1>(x)); x = fmaxf(x, dpp<XOR2>(x)); x = fmaxf(x, dpp<HMIR>(x)); return x; }
__device__ __forceinline__ float sum_hi8(float x) { x += dpp<ROR8>(x); x = tr16(x, x); x = tr32(x, x); return x; }
__device__ __forceinline__ float sum64(float x) { x = sum_row16(x); x = tr16(x, x); return tr32(x, x); }
__device__ __forceinline__ float max64(float x) { return max32(max16(max_row16(x))); }
__device__ __forceinline__ void unpack8(const v4u& w, float (&f)[8]) {
  f[0] = bflo(w.x); f[1] = bfhi(w.x); f[2] = bflo(w.y); f[3] = bfhi(w.y); f[4] = bflo(w.z); f[5] = bfhi(w.z); f[6] = bflo(w.w); f[7] = bfhi(w.w);
}

typedef __amdgpu_buffer_rsrc_t rsrc_t;
__device__ __forceinline__ rsrc_t mk(const void* p, unsigned bytes) { return __builtin_amdgcn_make_buffer_rsrc((void*)p, 0, (int)bytes, RSRC3); }
template <int POL> __device__ __forceinline__ v4u ld_p(rsrc_t r, int voff, int soff) { return __builtin_amdgcn_raw_buffer_load_b128(r, voff, soff, POL); }
#define ld_k ld_p<POL_K>
#define ld_v ld_p<POL_V>
#define ld_w ld_p<POL_W>
__device__ __forceinline__ v4u ld_g(rsrc_t r, int voff, int soff) { return __builtin_amdgcn_raw_buffer_load_b128(r, voff, soff, AUX_SC1); }
__device__ __forceinline__ void st_g(rsrc_t r, int voff, int soff, float a, float b, unsigned tA, unsigned tB) {
  v4u v = {__float_as_uint(a), tA, __float_as_uint(b), tB};
  __builtin_amdgcn_raw_buffer_store_b128(v, r, voff, soff, AUX_SC1);
}
__device__ __forceinline__ bool tag_ok(const v4u& g, unsigned tA, unsigned tB) { return g.y == tA && g.w == tB; }
template <int POL> __device__ __forceinline__ void glds(const void* gsrc, unsigned lds_dst) {
  unsigned keep;
  if (POL == 2)
    asm volatile("s_mov_b32 %0, m0\n\ts_mov_b32 m0, %2\n\ts_nop 0\n\tglobal_load_lds_dwordx4 %1, off nt\n\ts_mov_b32 m0, %0"
                 : "=&s"(keep) : "v"(gsrc), "s"(lds_dst) : "memory");
  else
    asm volatile("s_mov_b32 %0, m0\n\ts_mov_b32 m0, %2\n\ts_nop 0\n\tglobal_load_lds_dwordx4 %1, off\n\ts_mov_b32 m0, %0"
                 : "=&s"(keep) : "v"(gsrc), "s"(lds_dst) : "memory");
}
#define LDS_WAIT() asm volatile("s_waitcnt lgkmcnt(0)" ::: "memory")
#define VM_WAIT(n) asm volatile("s_waitcnt vmcnt(%0)" :: "i"(n) : "memory")
#define BAR() do { asm volatile("s_waitcnt lgkmcnt(0)" ::: "memory"); __builtin_amdgcn_s_barrier(); asm volatile("" ::: "memory"); } while (0)
#define SCHED() __builtin_amdgcn_sched_barrier(0)

template <int N, int SLP, typename OFF>
__device__ __forceinline__ unsigned poll(rsrc_t rs, int voff, OFF soff, bool active, unsigned tA, unsigned tB, v4u (&g)[N]) {
  unsigned spins = 0;
  for (; spins < SPIN_CAP; ++spins) {
    asm volatile("" ::: "memory");
    bool ok = true;
#pragma unroll
    for (int k = 0; k < N; ++k) g[k] = ld_g(rs, voff, soff(k));
#pragma unroll
    for (int k = 0; k < N; ++k) ok = ok && tag_ok(g[k], tA, tB);
    if (__all(ok || !active)) break;
    if (SLP) __builtin_amdgcn_s_sleep(SLP);
  }
  return spins;
}
template <int SLP>
__device__ __forceinline__ unsigned poll2(rsrc_t rs, int voff0, int soff0, int voff1, int soff1, unsigned tA, unsigned tB, v4u& g0, v4u& g1) {
  unsigned spins = 0;
  for (; spins < SPIN_CAP; ++spins) {
    asm volatile("" ::: "memory");
    g0 = ld_g(rs, voff0, soff0);
    g1 = ld_g(rs, voff1, soff1);
    if (__all(tag_ok(g0, tA, tB) && tag_ok(g1, tA, tB))) break;
    if (SLP) __builtin_amdgcn_s_sleep(SLP);
  }
  return spins;
}

__device__ __forceinline__ u64 rt() { return __builtin_amdgcn_s_memrealtime(); }
#define STAMPV(n, v) (void)(v)
#define STAMP(n)

template <int KWIN, int VP, int VL, int PSL, int D1, int D2, int D3, int D4, int TOUCH>
__global__ void __launch_bounds__(1024, 4)
adaln_decode_kernel(const u16* __restrict__ x, const u16* __restrict__ Kc, const u16* __restrict__ Vc, const u16* __restrict__ wq,
                    const u16* __restrict__ wo, const u16* __restrict__ wg, const u16* __restrict__ wu, const u16* __restrict__ wd,
                    const u16* __restrict__ cnd, const u16* __restrict__ wmod, u16* __restrict__ out, unsigned char* __restrict__ ws) {
  constexpr int VR = 16 - VP - VL;
  static_assert(VR >= 0 && VR <= 12, "");
  __shared__ __attribute__((aligned(16))) float lds[L_VT + 16 * (VL > 0 ? VL : 1) * 256];
  const int tid = threadIdx.x, lane = tid & 63, w = __builtin_amdgcn_readfirstlane(tid >> 6);
  const int b = blockIdx.x;
  const int h = (b & 7) * 4 + (b >> 6), s = (b >> 3) & 7;
  const int rho = lane >> 4, c16 = lane & 15;
  STAMP(0);
  volatile LAS unsigned* qflag = (volatile LAS unsigned*)&lds[L_FLAG];
  if (tid == 0) *qflag = 0u;
  const rsrc_t rws = mk(ws, WS_END);
  const size_t kvbase = ((size_t)h * SK + s * 1024) * 256;
  const rsrc_t rk = mk((const char*)Kc + kvbase, 262144);
  const rsrc_t rv = mk((const char*)Vc + kvbase, 262144);
  const int rot = ((b >> 3) * 5 + (b & 7) * 3) & 15;
  auto toff = [&](int i) { return ((((i + rot) & 15) << 4) + w) << 10; };
  const uint2 ep = *(const uint2*)(ws + WS_EPOCH);
  const unsigned tA = ep.x + 1u, tB = ep.y + 1u;
  BAR();

  v4u kv[16], vp[VP > 0 ? VP : 1];
  const unsigned vt_lds = (unsigned)(uintptr_t)(LAS float*)&lds[L_VT] + (unsigned)w * (VL * 1024);
  const char* vgl = (const char*)Vc + kvbase + lane * 16;
  if (w == 0) {
    const int cb = b % 60, rq = b / 60;
    const bool has_mod = b < 240;
    {
      const rsrc_t rm = mk((const char*)wmod + (size_t)(has_mod ? rq : 0) * 160 * 7680 + (has_mod ? cb : 0) * 128, has_mod ? 160 * 7680 : 0);
      v4u t[20];
      const int voff = (lane >> 3) * 7680 + (lane & 7) * 16;
      const int r0 = (has_mod ? rq : 0) * 160;
      u16 cv[3];
#pragma unroll
      for (int k = 0; k < 3; ++k) { const int i = lane + 64 * k; cv[k] = cnd[r0 + (i < 160 ? i : 0)]; }
#pragma unroll
      for (int u = 0; u < 20; ++u) t[u] = ld_w(rm, voff, u * 8 * 7680);
      SCHED();
#pragma unroll
      for (int k = 0; k < 3; ++k) lds[L_SC + lane + 64 * k] = rbf(silu(bflo(cv[k])));
      LDS_WAIT();
      float acc[8];
#pragma unroll
      for (int j = 0; j < 8; ++j) acc[j] = 0.f;
#pragma unroll
      for (int u = 0; u < 20; ++u) {
        const float sv = lds[L_SC + 8 * u + (lane >> 3)];
        float f[8]; unpack8(t[u], f);
#pragma unroll
        for (int j = 0; j < 8; ++j) acc[j] = fmaf(sv, f[j], acc[j]);
      }
      float y[4];
#pragma unroll
      for (int j = 0; j < 4; ++j) y[j] = tr32(acc[j], acc[j + 4]);
      float z0 = tr16(y[0], y[2]), z1 = tr16(y[1], y[3]);
      z0 += dpp<ROR8>(z0); z1 += dpp<ROR8>(z1);
      const int gi = 4 * (lane & 7) + rho;
      if ((lane & 8) == 0) *(LAS v2f*)&lds[L_TMP + 2 * gi] = v2f{z0, z1};
      LDS_WAIT();
      if (has_mod && lane < 32) { const v2f v = *(LAS v2f*)&lds[L_TMP + 2 * lane]; st_g(rws, lane * 16, WS_MOD + (rq * 1920 + cb * 32) * 16, v.x, v.y, tA, tB); }
    }
    STAMP(1);
    {
      const char* gq = (const char*)wq + (size_t)(s * 80 + rho) * 8192 + h * 256 + c16 * 16;
      const unsigned dst = (unsigned)(uintptr_t)(LAS float*)&lds[L_PART];
#pragma unroll
      for (int u = 0; u < 20; ++u) glds<POL_W>(gq + (size_t)u * 4 * 8192, dst + u * 1024);
    }
    SCHED();
    float mean_b, den_b;
    {
      float xv[10];
#pragma unroll
      for (int k = 0; k < 5; ++k) { const unsigned u = ((const unsigned*)x)[lane + 64 * k]; xv[2 * k] = bflo(u); xv[2 * k + 1] = bfhi(u); }
      float sm = 0.f;
#pragma unroll
      for (int k = 0; k < 10; ++k) sm += xv[k];
      const float mean = sum64(sm) * (1.f / D);
      float vs = 0.f;
#pragma unroll
      for (int k = 0; k < 10; ++k) { const float d = xv[k] - mean; vs = fmaf(d, d, vs); }
      const float var = sum64(vs) * (1.f / D);
      mean_b = rbf(mean); den_b = rbf(sqrtf(rbf(rbf(var) + 1e-5f)));
    }
    const int j40 = lane < 40 ? lane : 0;
    const unsigned xs2 = ((const unsigned*)x)[40 * s + j40];
    if (lane < 40) *(LAS v2f*)&lds[L_XS + 2 * lane] = v2f{bflo(xs2), bfhi(xs2)};
    {
      v4u g[8];
      poll<8, PSL>(rws, j40 * 16, [&](int k) { return WS_MOD + ((k & 3) * 1920 + (k >> 2) * 320 + 40 * s) * 16; }, lane < 40, tA, tB, g);
      const float sa0 = rbf((__uint_as_float(g[0].x) + __uint_as_float(g[1].x)) + (__uint_as_float(g[2].x) + __uint_as_float(g[3].x)));
      const float sa1 = rbf((__uint_as_float(g[0].z) + __uint_as_float(g[1].z)) + (__uint_as_float(g[2].z) + __uint_as_float(g[3].z)));
      const float ca0 = rbf((__uint_as_float(g[4].x) + __uint_as_float(g[5].x)) + (__uint_as_float(g[6].x) + __uint_as_float(g[7].x)));
      const float ca1 = rbf((__uint_as_float(g[4].z) + __uint_as_float(g[5].z)) + (__uint_as_float(g[6].z) + __uint_as_float(g[7].z)));
      const float h0 = rbf(rbf(rbf(rbf(bflo(xs2) - mean_b) / den_b) * rbf(1.f + ca0)) + sa0);
      const float h1 = rbf(rbf(rbf(rbf(bfhi(xs2) - mean_b) / den_b) * rbf(1.f + ca1)) + sa1);
      if (lane < 40) *(LAS v2f*)&lds[L_HS + 2 * lane] = v2f{h0, h1};
      VM_WAIT(0);
      LDS_WAIT();
    }
    STAMP(2);
    {
      float acc[8];
#pragma unroll
      for (int j = 0; j < 8; ++j) acc[j] = 0.f;
#pragma unroll
      for (int u = 0; u < 20; ++u) {
        const float hv = lds[L_HS + 4 * u + rho];
        const v4u tq = *(LAS v4u*)&lds[L_PART + u * 256 + lane * 4];
        float f[8]; unpack8(tq, f);
#pragma unroll
        for (int j = 0; j < 8; ++j) acc[j] = fmaf(hv, f[j], acc[j]);
      }
      float y[4];
#pragma unroll
      for (int j = 0; j < 4; ++j) y[j] = tr32(acc[j], acc[j + 4]);
      const float z0 = tr16(y[0], y[2]), z1 = tr16(y[1], y[3]);
      st_g(rws, lane * 16, WS_Q + (h * 8 + s) * 1024, z0, z1, tA, tB);
    }
    STAMP(3);
    {
      v4u g[8];
      poll<8, 0>(rws, lane * 16, [&](int k) { return WS_Q + (h * 8 + k) * 1024; }, true, tA, tB, g);
      float q0 = 0.f, q1 = 0.f;
#pragma unroll
      for (int k = 0; k < 8; ++k) { q0 += __uint_as_float(g[k].x); q1 += __uint_as_float(g[k].z); }
      ((LAS unsigned*)lds)[L_Q + 4 * c16 + rho] = bfbits(q0) | (bfbits(q1) << 16);
    }
    LDS_WAIT();
    *qflag = 1u;
    STAMP(4);
#pragma unroll
    for (int i = 0; i < 16; ++i) kv[i] = ld_k(rk, lane * 16, toff(i));
    SCHED();
#pragma unroll
    for (int i = 0; i < VP; ++i) vp[i] = ld_v(rv, lane * 16, toff(i));
#pragma unroll
    for (int i = 0; i < VL; ++i) glds<POL_V>(vgl + toff(VP + i), vt_lds + i * 1024);
    SCHED();
#pragma unroll
    for (int ch = 2; ch < 6; ++ch) {
      v4u g[4];
      poll<4, 1>(rws, j40 * 16, [&](int k) { return WS_MOD + (k * 1920 + ch * 320 + 40 * s) * 16; }, lane < 40, tA, tB, g);
      const float m0 = rbf((__uint_as_float(g[0].x) + __uint_as_float(g[1].x)) + (__uint_as_float(g[2].x) + __uint_as_float(g[3].x)));
      const float m1 = rbf((__uint_as_float(g[0].z) + __uint_as_float(g[1].z)) + (__uint_as_float(g[2].z) + __uint_as_float(g[3].z)));
      if (lane < 40) *(LAS v2f*)&lds[L_MOD + ch * 80 + 2 * lane] = v2f{m0, m1};
    }
    VM_WAIT(0);
  } else {
    if (TOUCH && w == 15) {
      const int g_ = h < 27 ? h : 0;
      int o0;
      if (lane < 9) o0 = WS_O + (h * 8 + s) * 1280 + lane * 128;
      else if (lane < 19) o0 = WS_Y + (s * 10 * 32 + h) * 64 + (lane - 9) * 2048;
      else if (lane < 27) o0 = WS_A + (g_ * 512 + s * 8) * 16 + (lane - 19) * 1024;
      else if (lane < 35) o0 = WS_B + (g_ * 512 + s * 8) * 16 + (lane - 27) * 1024;
      else if (lane < 43) o0 = WS_C + (g_ * 512 + s * 8) * 16 + (lane - 35) * 1024;
      else o0 = WS_S + (g_ * 8 + s) * 128;
      const unsigned t0_ = __builtin_amdgcn_raw_buffer_load_b32(rws, o0, 0, TOUCH == 1 ? AUX_SC1 : 0);
      const unsigned t1_ = __builtin_amdgcn_raw_buffer_load_b32(rws, (lane < 40 ? lane : 0) * 128, WS_D + (g_ * 8 + s) * 5120, TOUCH == 1 ? AUX_SC1 : 0);
      asm volatile("" :: "v"(t0_), "v"(t1_));
    }
#pragma unroll
    for (int i = 0; i < 16; ++i) {
      kv[i] = ld_k(rk, lane * 16, toff(i));
      if (i >= KWIN - 1) VM_WAIT(KWIN - 1);
      SCHED();
    }
#pragma unroll
    for (int i = 0; i < VP; ++i) { vp[i] = ld_v(rv, lane * 16, toff(i)); VM_WAIT(KWIN - 1); SCHED(); }
#pragma unroll
    for (int i = 0; i < VL; ++i) { glds<POL_V>(vgl + toff(VP + i), vt_lds + i * 1024); if (i < VL - 1) VM_WAIT(KWIN - 1); SCHED(); }
    STAMP(4);
    for (unsigned spins = 0; spins < (SPIN_CAP << 6); ++spins) { if (*qflag != 0u) break; __builtin_amdgcn_s_sleep(1); }
    asm volatile("" ::: "memory");
  }
  STAMP(5);
  const bool ffn = h < 27;
  const bool gw = ffn && w < 10, dw = ffn && w >= 10 && w < 15;
  const rsrc_t ro = mk((const char*)wo + (size_t)(h * 128 + 8 * w) * 1280 + s * 160, 8 * 1280);
  const int voff_o = c16 < 10 ? rho * 1280 + c16 * 16 : 0x7fffff00;
  const size_t goff = (size_t)(80 * s + 8 * (gw ? w : 0)) * 3456 + (gw ? h : 0) * 128;
  const rsrc_t rf0 = gw ? mk((const char*)wg + goff, 8 * 3456) : mk((const char*)wd + (size_t)(dw ? 64 * h + 8 * s : 0) * 1280, dw ? 8 * 1280 : 0);
  const rsrc_t rf1 = gw ? mk((const char*)wu + goff, 8 * 3456) : mk((const char*)wd + (size_t)(dw ? 64 * h + 8 * s : 0) * 1280 + 1280, dw ? 7 * 1280 : 0);
  const int voff_f = gw ? (lane >> 3) * 3456 + (lane & 7) * 16 : (lane & 3) * 2560 + ((dw ? w - 10 : 0) * 16 + (lane >> 2)) * 16;
  v4u wot[2], fwt[2];
  const v4u qf = *(LAS v4u*)&lds[L_Q + 4 * c16];
  float sc;
  {
    float xk[16];
#pragma unroll
    for (int i = 0; i < 16; ++i) {
      float d = dot2(kv[i].x, qf.x, 0.f); d = dot2(kv[i].y, qf.y, d); d = dot2(kv[i].z, qf.z, d); d = dot2(kv[i].w, qf.w, d);
      xk[i] = d;
      asm volatile("" : "+v"(xk[i]));
      if (i == 0) wot[0] = ld_w(ro, voff_o, 0);
      if (i == 1) wot[1] = ld_w(ro, voff_o, 4 * 1280);
      SCHED();
    }
#pragma unroll
    for (int j = 0; j < 8; ++j) xk[j] = trd<ROR8>(xk[j], xk[j + 8], (lane & 8) != 0);
#pragma unroll
    for (int j = 0; j < 4; ++j) xk[j] = trd<HMIR>(xk[j], xk[j + 4], (lane & 4) != 0);
#pragma unroll
    for (int j = 0; j < 2; ++j) xk[j] = trd<XOR2>(xk[j], xk[j + 2], (lane & 2) != 0);
    const float dot = trd<XOR1>(xk[0], xk[1], (lane & 1) != 0);
    sc = rbf(rbf(dot) * 0.08838834764831845f);
  }
  const float mw = max64(sc);
  const float p = ex2((sc - mw) * LOG2E);
  { const float lw = sum64(p); if (lane == 0) { lds[L_WM + w] = mw; lds[L_WL + w] = lw; } }
  STAMP(6);
  float acc[8];
#pragma unroll
  for (int j = 0; j < 8; ++j) acc[j] = 0.f;
#define PV_ACC(i, reg) { const float pb = dpp<BCAST + (i)>(p); float f[8]; unpack8(reg, f); \
    _Pragma("unroll") for (int j = 0; j < 8; ++j) acc[j] = fmaf(pb, f[j], acc[j]); }
  VM_WAIT(1); fwt[0] = ld_w(rf0, voff_f, 0); SCHED();
  VM_WAIT(1); fwt[1] = ld_w(rf1, voff_f, 0); SCHED();
#define PV_LANDED(i) if ((i) < VP) PV_ACC(i, vp[(i) < VP ? (i) : 0]) \
    else if ((i) < VP + VL) { const v4u t_ = *(LAS v4u*)&lds[L_VT + (w * VL + ((i) - VP)) * 256 + lane * 4]; PV_ACC(i, t_) }
  PV_LANDED(0) PV_LANDED(1) PV_LANDED(2) PV_LANDED(3) PV_LANDED(4) PV_LANDED(5) PV_LANDED(6) PV_LANDED(7)
  PV_LANDED(8) PV_LANDED(9) PV_LANDED(10) PV_LANDED(11) PV_LANDED(12) PV_LANDED(13) PV_LANDED(14) PV_LANDED(15)
#undef PV_LANDED
  SCHED();
#pragma unroll
  for (int i = 0; i < VR; ++i) { VM_WAIT(KWIN - 1); kv[i] = ld_v(rv, lane * 16, toff(VP + VL + i)); SCHED(); }
#define PV_LATE(i) if ((i) >= VP + VL) PV_ACC(i, kv[(i) >= VP + VL ? (i) - VP - VL : 0])
  PV_LATE(0) PV_LATE(1) PV_LATE(2) PV_LATE(3) PV_LATE(4) PV_LATE(5) PV_LATE(6) PV_LATE(7)
  PV_LATE(8) PV_LATE(9) PV_LATE(10) PV_LATE(11) PV_LATE(12) PV_LATE(13) PV_LATE(14) PV_LATE(15)
#undef PV_LATE
#undef PV_ACC
  STAMP(7);
  {
    float y[4];
#pragma unroll
    for (int j = 0; j < 4; ++j) y[j] = tr32(acc[j], acc[j + 4]);
    const float z0 = tr16(y[0], y[2]), z1 = tr16(y[1], y[3]);
    *(LAS v2f*)&lds[L_PART + w * 128 + 8 * c16 + 2 * rho] = v2f{z0, z1};
  }
  BAR();
  STAMP(8);
  const int r8 = lane >> 3;
  float gf[8], uf[8];
  float bmul = 0.f;
  if (w == 15) {
    __builtin_amdgcn_s_setprio(3);
    const int hi = lane >> 5;
    const float wm_a = lds[L_WM + c16], wm_b = lds[L_WM + (c16 & 7) + 8 * hi], wl_a = lds[L_WL + c16];
    v4f pv[8];
#pragma unroll
    for (int k = 0; k < 8; ++k) pv[k] = *(LAS v4f*)&lds[L_PART + (k + 8 * hi) * 128 + 4 * (lane & 31)];
    SCHED();
    const float Mcu = max_row16(wm_a);
    const float fk = ex2((wm_b - Mcu) * LOG2E);
    const float Lcu = sum_row16(ex2((wm_a - Mcu) * LOG2E) * wl_a);
    v4f o4 = {0.f, 0.f, 0.f, 0.f};
#define MRG(k) { const float f = dpp<BCAST + (k)>(fk); o4 += f * pv[k]; }
    MRG(0) MRG(1) MRG(2) MRG(3) MRG(4) MRG(5) MRG(6) MRG(7)
#undef MRG
    st_g(rws, lane * 16, WS_O + (h * 8 + s) * 1280, tr32(o4.x, o4.z), tr32(o4.y, o4.w), tA, tB);
    if (lane == 0) st_g(rws, 64 * 16, WS_O + (h * 8 + s) * 1280, Mcu, Lcu, tA, tB);
    STAMP(9);
    if (D1) __builtin_amdgcn_s_sleep(D1);
    v4u go[8], gml;
    for (unsigned spins = 0; spins < SPIN_CAP; ++spins) {
      asm volatile("" ::: "memory");
#pragma unroll
      for (int k = 0; k < 8; ++k) go[k] = ld_g(rws, lane * 16, WS_O + (h * 8 + k) * 1280);
      gml = ld_g(rws, (lane & 7) * 1280 + 64 * 16, WS_O + h * 8 * 1280);
      unsigned bad = (gml.y ^ tA) | (gml.w ^ tB);
#pragma unroll
      for (int k = 0; k < 8; ++k) bad |= (go[k].y ^ tA) | (go[k].w ^ tB);
      if (__all(bad == 0u)) break;
      __builtin_amdgcn_s_sleep(2);
    }
    STAMP(10);
    const float m = __uint_as_float(gml.x), l = __uint_as_float(gml.z);
    const float M = max_lo8(m);
    const float f = ex2((m - M) * LOG2E);
    const float rL = rcp(sum_lo8(f * l));
    float o0 = 0.f, o1 = 0.f;
#define CMB(k) { const float fb = __uint_as_float(__builtin_amdgcn_readlane(__float_as_uint(f), k)); o0 = fmaf(fb, __uint_as_float(go[k].x), o0); o1 = fmaf(fb, __uint_as_float(go[k].z), o1); }
    CMB(0) CMB(1) CMB(2) CMB(3) CMB(4) CMB(5) CMB(6) CMB(7)
#undef CMB
    *(LAS v2f*)&lds[L_OS + 4 * (lane & 31) + 2 * hi] = v2f{rbf(o0 * rL), rbf(o1 * rL)};
  } else if (gw) {
    unpack8(fwt[0], gf); unpack8(fwt[1], uf);
    bmul = rbf(1.f + lds[L_MOD + 4 * 80 + 8 * w + r8]);
    const float cmul = lds[L_MOD + 3 * 80 + 8 * w + r8];
#pragma unroll
    for (int t = 0; t < 2; ++t) {
      const float mul = t == 0 ? bmul : cmul;
      float y[8];
#pragma unroll
      for (int j = 0; j < 8; ++j) y[j] = tr32(mul * gf[j], mul * uf[j]);
      float z[4];
#pragma unroll
      for (int j = 0; j < 4; ++j) z[j] = tr16(y[j], y[j + 4]);
      const float e0 = trd<ROR8>(z[0], z[2], (lane & 8) != 0), e1 = trd<ROR8>(z[1], z[3], (lane & 8) != 0);
      *(LAS v2f*)&lds[(t == 0 ? L_PB : L_PC) + w * 128 + (lane >> 5) * 64 + 8 * (lane & 7) + 4 * ((lane >> 4) & 1) + 2 * ((lane >> 3) & 1)] = v2f{e0, e1};
    }
  }
  BAR();
  STAMP(11);
  {
    const float o0 = lds[L_OS + 8 * w + rho], o1 = lds[L_OS + 8 * w + 4 + rho];
    float f0[8], f1[8]; unpack8(wot[0], f0); unpack8(wot[1], f1);
    float a[8];
#pragma unroll
    for (int j = 0; j < 8; ++j) a[j] = fmaf(o0, f0[j], o1 * f1[j]);
    float y[4];
#pragma unroll
    for (int j = 0; j < 4; ++j) y[j] = tr32(a[j], a[j + 4]);
    const float z0 = tr16(y[0], y[2]), z1 = tr16(y[1], y[3]);
    if (c16 < 10) *(LAS v2f*)&lds[L_PY + w * 80 + 8 * c16 + 2 * rho] = v2f{z0, z1};
  }
  STAMP(12);
  BAR();
  STAMP(13);
  const int j40 = lane < 40 ? lane : 0;
  if (w == 15) {
    v2f pv[16];
#pragma unroll
    for (int k = 0; k < 16; ++k) pv[k] = *(LAS v2f*)&lds[L_PY + k * 80 + 2 * j40];
    SCHED();
    v2f ya = (pv[0] + pv[1]) + (pv[2] + pv[3]), yb = (pv[4] + pv[5]) + (pv[6] + pv[7]), yc = (pv[8] + pv[9]) + (pv[10] + pv[11]), yd = (pv[12] + pv[13]) + (pv[14] + pv[15]);
    ya = (ya + yb) + (yc + yd);
    const float y0 = ya.x, y1 = ya.y;
    if (lane < 40) st_g(rws, ((lane >> 2) * 32 * 4 + (lane & 3)) * 16, WS_Y + (s * 10 * 32 + h) * 64, y0, y1, tA, tB);
    STAMP(14);
  }
  if (ffn) {
    const int g = h, r = s;
    const int sig = ((lane & 7) >> 1) | ((lane & 1) << 2);
    if (w == 10 || w == 11) {
      const int pb = w == 10 ? L_PB : L_PC;
      float a0 = 0.f, a1 = 0.f;
#pragma unroll
      for (int k = 0; k < 10; ++k) { a0 += lds[pb + k * 128 + lane]; a1 += lds[pb + k * 128 + 64 + lane]; }
      st_g(rws, ((lane >> 3) * 64 + sig) * 16, (w == 10 ? WS_B : WS_C) + (g * 512 + r * 8) * 16, a0, a1, tA, tB);
    }
    float Bg = 0.f, Bu = 0.f, Cg = 0.f, Cu = 0.f;
    if (w == 10) {
      __builtin_amdgcn_s_sleep(16);
      v4u gb[2];
      poll<2, 8>(rws, lane * 16, [&](int k) { return (k == 0 ? WS_B : WS_C) + (g * 8 + r) * 1024; }, true, tA, tB, gb);
      Bg = sum_hi8(__uint_as_float(gb[0].x)); Bu = sum_hi8(__uint_as_float(gb[0].z));
      Cg = sum_hi8(__uint_as_float(gb[1].x)); Cu = sum_hi8(__uint_as_float(gb[1].z));
    }
    if (w < 10) {
      if (D2) __builtin_amdgcn_s_sleep(D2);
      v4u gy[2];
      const unsigned np2 = poll<2, PSL>(rws, c16 * 64 + rho * 16, [&](int k) { return WS_Y + ((r * 10 + w) * 32 + 16 * k) * 64; }, true, tA, tB, gy);
      STAMPV(26, np2);
      STAMP(16);
      const float y0 = sum_row16(__uint_as_float(gy[0].x) + __uint_as_float(gy[1].x));
      const float y1 = sum_row16(__uint_as_float(gy[0].z) + __uint_as_float(gy[1].z));
      const float attn = rbf((lane & 8) ? y1 : y0);
      const int cl = 8 * w + r8;
      const float x1 = rbf(lds[L_XS + cl] + rbf(lds[L_MOD + 2 * 80 + cl] * attn));
      const float xs = x1 * bmul;
      float y[8];
#pragma unroll
      for (int j = 0; j < 8; ++j) y[j] = tr32(xs * gf[j], xs * uf[j]);
      float z[4];
#pragma unroll
      for (int j = 0; j < 4; ++j) z[j] = tr16(y[j], y[j + 4]);
      const float e0 = trd<ROR8>(z[0], z[2], (lane & 8) != 0), e1 = trd<ROR8>(z[1], z[3], (lane & 8) != 0);
      *(LAS v2f*)&lds[L_PA + w * 128 + (lane >> 5) * 64 + 8 * (lane & 7) + 4 * ((lane >> 4) & 1) + 2 * ((lane >> 3) & 1)] = v2f{e0, e1};
      const float mean_w = sum_hi8(x1) * 0.125f;
      const float dd = x1 - mean_w;
      const float m2_w = sum_hi8(dd * dd);
      if (lane == 0) *(LAS v2f*)&lds[L_STAT + 2 * w] = v2f{mean_w, m2_w};
      if ((lane & 7) == 0) lds[L_X1 + cl] = x1;
    }
    STAMP(17);
    BAR();
    STAMP(18);
    if (w == 0) {
      float pa[10], pb_[10];
#pragma unroll
      for (int k = 0; k < 10; ++k) { pa[k] = lds[L_PA + k * 128 + lane]; pb_[k] = lds[L_PA + k * 128 + 64 + lane]; }
      SCHED();
      const float a0 = ((pa[0] + pa[1]) + (pa[2] + pa[3])) + ((pa[4] + pa[5]) + (pa[6] + pa[7])) + (pa[8] + pa[9]);
      const float a1 = ((pb_[0] + pb_[1]) + (pb_[2] + pb_[3])) + ((pb_[4] + pb_[5]) + (pb_[6] + pb_[7])) + (pb_[8] + pb_[9]);
      st_g(rws, ((lane >> 3) * 64 + sig) * 16, WS_A + (g * 512 + r * 8) * 16, a0, a1, tA, tB);
      STAMP(19);
    } else if (w == 1) {
      const v2f sv = *(LAS v2f*)&lds[L_STAT + 2 * (c16 < 10 ? c16 : 0)];
      const float mk_ = c16 < 10 ? sv.x : 0.f;
      const float mean_r = sum_row16(mk_) * 0.1f;
      const float dd = sv.x - mean_r;
      const float m2 = sum_row16(c16 < 10 ? fmaf(8.f * dd, dd, sv.y) : 0.f);
      if (lane == 0) st_g(rws, 0, WS_S + (g * 8 + r) * 128, mean_r, m2, tA, tB);
      STAMP(15);
    } else if (w == 10) {
      if (D3) __builtin_amdgcn_s_sleep(D3);
      v4u ga, gs;
      STAMP(25);
      const unsigned np3 = poll2<1>(rws, lane * 16, WS_A + (g * 8 + r) * 1024, (lane >> 3) * 128, WS_S + g * 1024, tA, tB, ga, gs);
      STAMPV(27, np3);
      STAMP(20);
      const float Ag = sum_hi8(__uint_as_float(ga.x)), Au = sum_hi8(__uint_as_float(ga.z));
      const float mr = __uint_as_float(gs.x);
      const float mean = sum_hi8(mr) * 0.125f;
      const float dd = mr - mean;
      const float var = sum_hi8(fmaf(80.f * dd, dd, __uint_as_float(gs.z))) * (1.f / D);
      const float mb = rbf(mean), den = rbf(__builtin_amdgcn_sqrtf(rbf(rbf(var) + 1e-5f)));
      const float rden = rcp(den);
      const float gate = rbf(fmaf(Ag - mb * Bg, rden, Cg)), up = rbf(fmaf(Au - mb * Bu, rden, Cu));
      const float act = rbf(rbf(silu_fast(gate)) * up);
      if (lane < 8) lds[L_ACT + 2 * (lane & 3) + (lane >> 2)] = act;
    }
    BAR();
    STAMP(21);
    if (dw) {
      const v2f av = *(LAS v2f*)&lds[L_ACT + 2 * (lane & 3)];
      float f0[8], f1[8]; unpack8(fwt[0], f0); unpack8(fwt[1], f1);
      float d[8];
#pragma unroll
      for (int j = 0; j < 8; ++j) d[j] = fmaf(av.x, f0[j], av.y * f1[j]);
      float y[4];
#pragma unroll
      for (int j = 0; j < 4; ++j) y[j] = trd<XOR2>(d[j], d[j + 4], (lane & 2) != 0);
      const float e0 = trd<XOR1>(y[0], y[2], (lane & 1) != 0), e1 = trd<XOR1>(y[1], y[3], (lane & 1) != 0);
      st_g(rws, lane * 16, WS_D + ((g * 8 + r) * 320 + (w - 10) * 64) * 16, e0, e1, tA, tB);
      STAMP(22);
    }
    if (g < 5) {
      if (D4) __builtin_amdgcn_s_sleep(D4);
      v4u gd[2];
      const int n1 = w + 16 < 27 ? w + 16 : w;
      const int voff = (lane >> 3) * 5120 + (lane & 7) * 16;
      STAMP(23);
      const unsigned np4 = poll<2, 1>(rws, voff, [&](int k) { return WS_D + (k == 0 ? w : n1) * 8 * 5120 + (40 * r + 8 * g) * 16; }, true, tA, tB, gd);
      STAMPV(28, np4);
      STAMP(24);
      float e0 = __uint_as_float(gd[0].x), e1 = __uint_as_float(gd[0].z);
      if (w + 16 < 27) { e0 += __uint_as_float(gd[1].x); e1 += __uint_as_float(gd[1].z); }
      e0 = sum_hi8(e0); e1 = sum_hi8(e1);
      if (lane < 8) *(LAS v2f*)&lds[L_PD + w * 16 + 2 * lane] = v2f{e0, e1};
      BAR();
      if (w == 0 && lane < 8) {
        v2f pv[16];
#pragma unroll
        for (int k = 0; k < 16; ++k) pv[k] = *(LAS v2f*)&lds[L_PD + k * 16 + 2 * lane];
        const int cl = 16 * g + 2 * lane;
        const v2f x1v = *(LAS v2f*)&lds[L_X1 + cl], gbv = *(LAS v2f*)&lds[L_MOD + 5 * 80 + cl];
        SCHED();
        v2f ya = (pv[0] + pv[1]) + (pv[2] + pv[3]), yb = (pv[4] + pv[5]) + (pv[6] + pv[7]), yc = (pv[8] + pv[9]) + (pv[10] + pv[11]), yd = (pv[12] + pv[13]) + (pv[14] + pv[15]);
        ya = (ya + yb) + (yc + yd);
        const float f0 = ya.x, f1 = ya.y;
        const float o0 = x1v.x + rbf(gbv.x * rbf(f0));
        const float o1 = x1v.y + rbf(gbv.y * rbf(f1));
        ((unsigned*)out)[(80 * r + cl) >> 1] = bfbits(o0) | (bfbits(o1) << 16);
        if (b == 0 && lane == 0) *(uint2*)(ws + WS_EPOCH) = make_uint2(tA, tB);
      }
    }
  }
  STAMP(30);
}

template <int KWIN, int VP, int VL, int PSL, int D1, int D2, int D3, int D4, int TOUCH>
static void launch(void* const* d_in, void* d_out, void* d_ws, hipStream_t stream) {
  adaln_decode_kernel<KWIN, VP, VL, PSL, D1, D2, D3, D4, TOUCH><<<256, 1024, 0, stream>>>(
      (const u16*)d_in[0], (const u16*)d_in[1], (const u16*)d_in[2], (const u16*)d_in[3], (const u16*)d_in[4], (const u16*)d_in[6],
      (const u16*)d_in[7], (const u16*)d_in[8], (const u16*)d_in[9], (const u16*)d_in[10], (u16*)d_out, (unsigned char*)d_ws);
}

}

extern "C" void kernel_launch(void* const* d_in, const int* in_sizes, int n_in, void* d_out, int out_size, void* d_ws, size_t ws_size,
                              hipStream_t stream) {
  adaln::launch<2, 6, 6, 8, 8, 8, 8, 8, 2>(d_in, d_out, d_ws, stream);
}
```

```cpp
#include <hip/hip_runtime.h>
#include <stdint.h>

#define DBG_STAMPS 0

namespace adaln {

typedef unsigned short u16;
typedef unsigned long long u64;
typedef unsigned v4u __attribute__((ext_vector_type(4)));
typedef float v2f __attribute__((ext_vector_type(2)));
typedef float v4f __attribute__((ext_vector_type(4)));
typedef __bf16 bf16x2 __attribute__((ext_vector_type(2)));
#define LAS __attribute__((address_space(3)))

constexpr int D = 640, SK = 8192, F = 1728;
constexpr int AUX_NT = 2, AUX_SC1 = 16;
constexpr int RSRC3 = 0x00020000;
constexpr float LOG2E = 1.4426950408889634f;
constexpr unsigned SPIN_CAP = 1u << 16;

constexpr int WS_EPOCH = 0;
constexpr int WS_MOD = 256;
constexpr int WS_Q = WS_MOD + 4 * 1920 * 16;
constexpr int WS_O = WS_Q + 32 * 8 * 64 * 16;
constexpr int WS_Y = WS_O + 32 * 8 * 80 * 16;
constexpr int WS_A = WS_Y + 8 * 10 * 32 * 4 * 16;
constexpr int WS_B = WS_A + 27 * 8 * 64 * 16;
constexpr int WS_C = WS_B + 27 * 8 * 64 * 16;
constexpr int WS_S = WS_C + 27 * 8 * 64 * 16;
constexpr int WS_D = WS_S + 27 * 8 * 128;
constexpr int WS_END = WS_D + 216 * 320 * 16;
constexpr int WS_DBG = 4 << 20;

constexpr int L_Q = 0;
constexpr int L_FLAG = L_Q + 64;
constexpr int L_HS = L_FLAG + 4;
constexpr int L_SC = L_HS + 80;
constexpr int L_TMP = L_SC + 192;
constexpr int L_MOD = L_TMP + 64;
constexpr int L_WM = L_MOD + 480;
constexpr int L_WL = L_WM + 16;
constexpr int L_STAT = L_WL + 16;
constexpr int L_X1 = L_STAT + 32;
constexpr int L_XS = L_X1 + 80;
constexpr int L_OS = L_XS + 80;
constexpr int L_ACT = L_OS + 128;
constexpr int L_PD = L_ACT + 16;
constexpr int L_PART = L_PD + 256;
constexpr int L_PY = L_PART + 2048;
constexpr int L_PA = L_PY + 1280;
constexpr int L_PB = L_PA + 1280;
constexpr int L_PC = L_PB + 1280;
constexpr int L_VT = L_PC + 1280;
static_assert(L_VT % 4 == 0 && L_PART % 4 == 0 && L_VT - L_PART >= 5120, "alignment; room for the wq tile");

__device__ __forceinline__ float bflo(unsigned u) { return __uint_as_float(u << 16); }
__device__ __forceinline__ float bfhi(unsigned u) { return __uint_as_float(u & 0xffff0000u); }
__device__ __forceinline__ float rbf(float f) { unsigned u; asm("v_cvt_pk_bf16_f32 %0, %1, %1" : "=v"(u) : "v"(f)); return __uint_as_float(u << 16); }
__device__ __forceinline__ unsigned bfbits(float f) { return (unsigned)__builtin_bit_cast(u16, (__bf16)f); }
__device__ __forceinline__ float ex2(float x) { return __builtin_amdgcn_exp2f(x); }
__device__ __forceinline__ float silu(float x) { return x / (1.f + ex2(-x * LOG2E)); }
__device__ __forceinline__ float rcp(float x) { return __builtin_amdgcn_rcpf(x); }
__device__ __forceinline__ float silu_fast(float x) { return x * rcp(1.f + ex2(-x * LOG2E)); }
__device__ __forceinline__ float dot2(unsigned a, unsigned b, float acc) {
  return __builtin_amdgcn_fdot2_f32_bf16(__builtin_bit_cast(bf16x2, a), __builtin_bit_cast(bf16x2, b), acc, false);
}
template <int CTRL> __device__ __forceinline__ float dpp(float x) {
  return __builtin_bit_cast(float, __builtin_amdgcn_mov_dpp(__builtin_bit_cast(int, x), CTRL, 0xf, 0xf, true));
}
constexpr int XOR1 = 0xB1, XOR2 = 0x4E, HMIR = 0x141, MIR = 0x140, ROR4 = 0x124, ROR8 = 0x128, BCAST = 0x150;
__device__ __forceinline__ float tr32(float a, float b) {
  auto r = __builtin_amdgcn_permlane32_swap(__float_as_uint(a), __float_as_uint(b), false, false);
  return __uint_as_float(r[0]) + __uint_as_float(r[1]);
}
__device__ __forceinline__ float tr16(float a, float b) {
  auto r = __builtin_amdgcn_permlane16_swap(__float_as_uint(a), __float_as_uint(b), false, false);
  return __uint_as_float(r[0]) + __uint_as_float(r[1]);
}
__device__ __forceinline__ float max32(float a) {
  auto r = __builtin_amdgcn_permlane32_swap(__float_as_uint(a), __float_as_uint(a), false, false);
  return fmaxf(__uint_as_float(r[0]), __uint_as_float(r[1]));
}
__device__ __forceinline__ float max16(float a) {
  auto r = __builtin_amdgcn_permlane16_swap(__float_as_uint(a), __float_as_uint(a), false, false);
  return fmaxf(__uint_as_float(r[0]), __uint_as_float(r[1]));
}
template <int CTRL> __device__ __forceinline__ float trd(float a, float b, bool hi) {
  const float t1 = a + dpp<CTRL>(a), t2 = b + dpp<CTRL>(b);
  return hi ? t2 : t1;
}
__device__ __forceinline__ float sum_row16(float x) { x += dpp<XOR1>(x); x += dpp<XOR2>(x); x += dpp<HMIR>(x); x += dpp<MIR>(x); return x; }
__device__ __forceinline__ float max_row16(float x) { x = fmaxf(x, dpp<XOR1>(x)); x = fmaxf(x, dpp<XOR2>(x)); x = fmaxf(x, dpp<HMIR>(x)); return fmaxf(x, dpp<MIR>(x)); }
__device__ __forceinline__ float sum_lo8(float x) { x += dpp<XOR1>(x); x += dpp<XOR2>(x); x += dpp<HMIR>(x); return x; }
__device__ __forceinline__ float max_lo8(float x) { x = fmaxf(x, dpp<XOR1>(x)); x = fmaxf(x, dpp<XOR2>(x)); x = fmaxf(x, dpp<HMIR>(x)); return x; }
__device__ __forceinline__ float sum_hi8(float x) { x += dpp<ROR8>(x); x = tr16(x, x); x = tr32(x, x); return x; }
__device__ __forceinline__ float sum64(float x) { x = sum_row16(x); x = tr16(x, x); return tr32(x, x); }
__device__ __forceinline__ float max64(float x) { return max32(max16(max_row16(x))); }
__device__ __forceinline__ void unpack8(const v4u& w, float (&f)[8]) {
  f[0] = bflo(w.x); f[1] = bfhi(w.x); f[2] = bflo(w.y); f[3] = bfhi(w.y); f[4] = bflo(w.z); f[5] = bfhi(w.z); f[6] = bflo(w.w); f[7] = bfhi(w.w);
}

typedef __amdgpu_buffer_rsrc_t rsrc_t;
__device__ __forceinline__ rsrc_t mk(const void* p, unsigned bytes) { return __builtin_amdgcn_make_buffer_rsrc((void*)p, 0, (int)bytes, RSRC3); }
__device__ __forceinline__ v4u ld_nt(rsrc_t r, int voff, int soff) { return __builtin_amdgcn_raw_buffer_load_b128(r, voff, soff, AUX_NT); }
__device__ __forceinline__ v4u ld_g(rsrc_t r, int voff, int soff) { return __builtin_amdgcn_raw_buffer_load_b128(r, voff, soff, AUX_SC1); }
__device__ __forceinline__ void st_g(rsrc_t r, int voff, int soff, float a, float b, unsigned tA, unsigned tB) {
  v4u v = {__float_as_uint(a), tA, __float_as_uint(b), tB};
  __builtin_amdgcn_raw_buffer_store_b128(v, r, voff, soff, AUX_SC1);
}
__device__ __forceinline__ bool tag_ok(const v4u& g, unsigned tA, unsigned tB) { return g.y == tA && g.w == tB; }
__device__ __forceinline__ void glds_nt(const void* gsrc, unsigned lds_dst) {
  unsigned keep;
  asm volatile("s_mov_b32 %0, m0\n\ts_mov_b32 m0, %2\n\ts_nop 0\n\tglobal_load_lds_dwordx4 %1, off nt\n\ts_mov_b32 m0, %0"
               : "=&s"(keep) : "v"(gsrc), "s"(lds_dst) : "memory");
}
#define LDS_WAIT() asm volatile("s_waitcnt lgkmcnt(0)" ::: "memory")
#define VM_WAIT(n) asm volatile("s_waitcnt vmcnt(%0)" :: "i"(n) : "memory")
#define BAR() do { asm volatile("s_waitcnt lgkmcnt(0)" ::: "memory"); __builtin_amdgcn_s_barrier(); asm volatile("" ::: "memory"); } while (0)
#define SCHED() __builtin_amdgcn_sched_barrier(0)

template <int N, int SLP, typename OFF>
__device__ __forceinline__ unsigned poll(rsrc_t rs, int voff, OFF soff, bool active, unsigned tA, unsigned tB, v4u (&g)[N]) {
  unsigned spins = 0;
  for (; spins < SPIN_CAP; ++spins) {
    asm volatile("" ::: "memory");
    bool ok = true;
#pragma unroll
    for (int k = 0; k < N; ++k) g[k] = ld_g(rs, voff, soff(k));
#pragma unroll
    for (int k = 0; k < N; ++k) ok = ok && tag_ok(g[k], tA, tB);
    if (__all(ok || !active)) break;
    if (SLP) __builtin_amdgcn_s_sleep(SLP);
  }
  return spins;
}
template <int SLP>
__device__ __forceinline__ unsigned poll2(rsrc_t rs, int voff0, int soff0, int voff1, int soff1, unsigned tA, unsigned tB, v4u& g0, v4u& g1) {
  unsigned spins = 0;
  for (; spins < SPIN_CAP; ++spins) {
    asm volatile("" ::: "memory");
    g0 = ld_g(rs, voff0, soff0);
    g1 = ld_g(rs, voff1, soff1);
    if (__all(tag_ok(g0, tA, tB) && tag_ok(g1, tA, tB))) break;
    if (SLP) __builtin_amdgcn_s_sleep(SLP);
  }
  return spins;
}

__device__ __forceinline__ u64 rt() { return __builtin_amdgcn_s_memrealtime(); }
#define STAMPV(n, v) (void)(v)
#define STAMP(n)

template <int KWIN, int VP, int VL, int PSL, int D1, int D2, int D3, int D4, int TOUCH>
__global__ void __launch_bounds__(1024, 4)
adaln_decode_kernel(const u16* __restrict__ x, const u16* __restrict__ Kc, const u16* __restrict__ Vc, const u16* __restrict__ wq,
                    const u16* __restrict__ wo, const u16* __restrict__ wg, const u16* __restrict__ wu, const u16* __restrict__ wd,
                    const u16* __restrict__ cnd, const u16* __restrict__ wmod, u16* __restrict__ out, unsigned char* __restrict__ ws) {
  constexpr int VR = 16 - VP - VL;
  static_assert(VR >= 0 && VR <= 12, "");
  __shared__ __attribute__((aligned(16))) float lds[L_VT + 16 * (VL > 0 ? VL : 1) * 256];
  const int tid = threadIdx.x, lane = tid & 63, w = __builtin_amdgcn_readfirstlane(tid >> 6);
  const int b = blockIdx.x;
  const int h = (b & 7) * 4 + (b >> 6), s = (b >> 3) & 7;
  const int rho = lane >> 4, c16 = lane & 15;
  STAMP(0);
  volatile LAS unsigned* qflag = (volatile LAS unsigned*)&lds[L_FLAG];
  if (tid == 0) *qflag = 0u;
  const rsrc_t rws = mk(ws, WS_END);
  const size_t kvbase = ((size_t)h * SK + s * 1024) * 256;
  const rsrc_t rk = mk((const char*)Kc + kvbase, 262144);
  const rsrc_t rv = mk((const char*)Vc + kvbase, 262144);
  const int rot = ((b >> 3) * 5 + (b & 7) * 3) & 15;
  auto toff = [&](int i) { return ((((i + rot) & 15) << 4) + w) << 10; };
  const uint2 ep = *(const uint2*)(ws + WS_EPOCH);
  const unsigned tA = ep.x + 1u, tB = ep.y + 1u;
  BAR();

  v4u kv[16], vp[VP > 0 ? VP : 1];
  const unsigned vt_lds = (unsigned)(uintptr_t)(LAS float*)&lds[L_VT] + (unsigned)w * (VL * 1024);
  const char* vgl = (const char*)Vc + kvbase + lane * 16;
  if (w == 0) {
    const int cb = b % 60, rq = b / 60;
    const bool has_mod = b < 240;
    {
      const rsrc_t rm = mk((const char*)wmod + (size_t)(has_mod ? rq : 0) * 160 * 7680 + (has_mod ? cb : 0) * 128, has_mod ? 160 * 7680 : 0);
      v4u t[20];
      const int voff = (lane >> 3) * 7680 + (lane & 7) * 16;
      const int r0 = (has_mod ? rq : 0) * 160;
      u16 cv[3];
#pragma unroll
      for (int k = 0; k < 3; ++k) { const int i = lane + 64 * k; cv[k] = cnd[r0 + (i < 160 ? i : 0)]; }
#pragma unroll
      for (int u = 0; u < 20; ++u) t[u] = ld_nt(rm, voff, u * 8 * 7680);
      SCHED();
#pragma unroll
      for (int k = 0; k < 3; ++k) lds[L_SC + lane + 64 * k] = rbf(silu(bflo(cv[k])));
      LDS_WAIT();
      float acc[8];
#pragma unroll
      for (int j = 0; j < 8; ++j) acc[j] = 0.f;
#pragma unroll
      for (int u = 0; u < 20; ++u) {
        const float sv = lds[L_SC + 8 * u + (lane >> 3)];
        float f[8]; unpack8(t[u], f);
#pragma unroll
        for (int j = 0; j < 8; ++j) acc[j] = fmaf(sv, f[j], acc[j]);
      }
      float y[4];
#pragma unroll
      for (int j = 0; j < 4; ++j) y[j] = tr32(acc[j], acc[j + 4]);
      float z0 = tr16(y[0], y[2]), z1 = tr16(y[1], y[3]);
      z0 += dpp<ROR8>(z0); z1 += dpp<ROR8>(z1);
      const int gi = 4 * (lane & 7) + rho;
      if ((lane & 8) == 0) *(LAS v2f*)&lds[L_TMP + 2 * gi] = v2f{z0, z1};
      LDS_WAIT();
      if (has_mod && lane < 32) { const v2f v = *(LAS v2f*)&lds[L_TMP + 2 * lane]; st_g(rws, lane * 16, WS_MOD + (rq * 1920 + cb * 32) * 16, v.x, v.y, tA, tB); }
    }
    STAMP(1);
    {
      const char* gq = (const char*)wq + (size_t)(s * 80 + rho) * 8192 + h * 256 + c16 * 16;
      const unsigned dst = (unsigned)(uintptr_t)(LAS float*)&lds[L_PART];
#pragma unroll
      for (int u = 0; u < 20; ++u) glds_nt(gq + (size_t)u * 4 * 8192, dst + u * 1024);
    }
    SCHED();
    float mean_b, den_b;
    {
      float xv[10];
#pragma unroll
      for (int k = 0; k < 5; ++k) { const unsigned u = ((const unsigned*)x)[lane + 64 * k]; xv[2 * k] = bflo(u); xv[2 * k + 1] = bfhi(u); }
      float sm = 0.f;
#pragma unroll
      for (int k = 0; k < 10; ++k) sm += xv[k];
      const float mean = sum64(sm) * (1.f / D);
      float vs = 0.f;
#pragma unroll
      for (int k = 0; k < 10; ++k) { const float d = xv[k] - mean; vs = fmaf(d, d, vs); }
      const float var = sum64(vs) * (1.f / D);
      mean_b = rbf(mean); den_b = rbf(sqrtf(rbf(rbf(var) + 1e-5f)));
    }
    const int j40 = lane < 40 ? lane : 0;
    const unsigned xs2 = ((const unsigned*)x)[40 * s + j40];
    if (lane < 40) *(LAS v2f*)&lds[L_XS + 2 * lane] = v2f{bflo(xs2), bfhi(xs2)};
    {
      v4u g[8];
      poll<8, PSL>(rws, j40 * 16, [&](int k) { return WS_MOD + ((k & 3) * 1920 + (k >> 2) * 320 + 40 * s) * 16; }, lane < 40, tA, tB, g);
      const float sa0 = rbf((__uint_as_float(g[0].x) + __uint_as_float(g[1].x)) + (__uint_as_float(g[2].x) + __uint_as_float(g[3].x)));
      const float sa1 = rbf((__uint_as_float(g[0].z) + __uint_as_float(g[1].z)) + (__uint_as_float(g[2].z) + __uint_as_float(g[3].z)));
      const float ca0 = rbf((__uint_as_float(g[4].x) + __uint_as_float(g[5].x)) + (__uint_as_float(g[6].x) + __uint_as_float(g[7].x)));
      const float ca1 = rbf((__uint_as_float(g[4].z) + __uint_as_float(g[5].z)) + (__uint_as_float(g[6].z) + __uint_as_float(g[7].z)));
      const float h0 = rbf(rbf(rbf(rbf(bflo(xs2) - mean_b) / den_b) * rbf(1.f + ca0)) + sa0);
      const float h1 = rbf(rbf(rbf(rbf(bfhi(xs2) - mean_b) / den_b) * rbf(1.f + ca1)) + sa1);
      if (lane < 40) *(LAS v2f*)&lds[L_HS + 2 * lane] = v2f{h0, h1};
      VM_WAIT(0);
      LDS_WAIT();
    }
    STAMP(2);
    {
      float acc[8];
#pragma unroll
      for (int j = 0; j < 8; ++j) acc[j] = 0.f;
#pragma unroll
      for (int u = 0; u < 20; ++u) {
        const float hv = lds[L_HS + 4 * u + rho];
        const v4u tq = *(LAS v4u*)&lds[L_PART + u * 256 + lane * 4];
        float f[8]; unpack8(tq, f);
#pragma unroll
        for (int j = 0; j < 8; ++j) acc[j] = fmaf(hv, f[j], acc[j]);
      }
      float y[4];
#pragma unroll
      for (int j = 0; j < 4; ++j) y[j] = tr32(acc[j], acc[j + 4]);
      const float z0 = tr16(y[0], y[2]), z1 = tr16(y[1], y[3]);
      st_g(rws, lane * 16, WS_Q + (h * 8 + s) * 1024, z0, z1, tA, tB);
    }
    STAMP(3);
    {
      v4u g[8];
      poll<8, 0>(rws, lane * 16, [&](int k) { return WS_Q + (h * 8 + k) * 1024; }, true, tA, tB, g);
      float q0 = 0.f, q1 = 0.f;
#pragma unroll
      for (int k = 0; k < 8; ++k) { q0 += __uint_as_float(g[k].x); q1 += __uint_as_float(g[k].z); }
      ((LAS unsigned*)lds)[L_Q + 4 * c16 + rho] = bfbits(q0) | (bfbits(q1) << 16);
    }
    LDS_WAIT();
    *qflag = 1u;
    STAMP(4);
#pragma unroll
    for (int i = 0; i < 16; ++i) kv[i] = ld_nt(rk, lane * 16, toff(i));
    SCHED();
#pragma unroll
    for (int i = 0; i < VP; ++i) vp[i] = ld_nt(rv, lane * 16, toff(i));
#pragma unroll
    for (int i = 0; i < VL; ++i) glds_nt(vgl + toff(VP + i), vt_lds + i * 1024);
    SCHED();
#pragma unroll
    for (int ch = 2; ch < 6; ++ch) {
      v4u g[4];
      poll<4, 1>(rws, j40 * 16, [&](int k) { return WS_MOD + (k * 1920 + ch * 320 + 40 * s) * 16; }, lane < 40, tA, tB, g);
      const float m0 = rbf((__uint_as_float(g[0].x) + __uint_as_float(g[1].x)) + (__uint_as_float(g[2].x) + __uint_as_float(g[3].x)));
      const float m1 = rbf((__uint_as_float(g[0].z) + __uint_as_float(g[1].z)) + (__uint_as_float(g[2].z) + __uint_as_float(g[3].z)));
      if (lane < 40) *(LAS v2f*)&lds[L_MOD + ch * 80 + 2 * lane] = v2f{m0, m1};
    }
    VM_WAIT(0);
  } else {
    if (TOUCH && w == 15) {
      const int g_ = h < 27 ? h : 0;
      int o0;
      if (lane < 9) o0 = WS_O + (h * 8 + s) * 1280 + lane * 128;
      else if (lane < 19) o0 = WS_Y + (s * 10 * 32 + h) * 64 + (lane - 9) * 2048;
      else if (lane < 27) o0 = WS_A + (g_ * 512 + s * 8) * 16 + (lane - 19) * 1024;
      else if (lane < 35) o0 = WS_B + (g_ * 512 + s * 8) * 16 + (lane - 27) * 1024;
      else if (lane < 43) o0 = WS_C + (g_ * 512 + s * 8) * 16 + (lane - 35) * 1024;
      else o0 = WS_S + (g_ * 8 + s) * 128;
      const unsigned t0_ = __builtin_amdgcn_raw_buffer_load_b32(rws, o0, 0, TOUCH == 1 ? AUX_SC1 : 0);
      const unsigned t1_ = __builtin_amdgcn_raw_buffer_load_b32(rws, (lane < 40 ? lane : 0) * 128, WS_D + (g_ * 8 + s) * 5120, TOUCH == 1 ? AUX_SC1 : 0);
      asm volatile("" :: "v"(t0_), "v"(t1_));
    }
#pragma unroll
    for (int i = 0; i < 16; ++i) {
      kv[i] = ld_nt(rk, lane * 16, toff(i));
      if (i >= KWIN - 1) VM_WAIT(KWIN - 1);
      SCHED();
    }
#pragma unroll
    for (int i = 0; i < VP; ++i) { vp[i] = ld_nt(rv, lane * 16, toff(i)); VM_WAIT(KWIN - 1); SCHED(); }
#pragma unroll
    for (int i = 0; i < VL; ++i) { glds_nt(vgl + toff(VP + i), vt_lds + i * 1024); if (i < VL - 1) VM_WAIT(KWIN - 1); SCHED(); }
    STAMP(4);
    for (unsigned spins = 0; spins < (SPIN_CAP << 6); ++spins) { if (*qflag != 0u) break; __builtin_amdgcn_s_sleep(1); }
    asm volatile("" ::: "memory");
  }
  STAMP(5);
  const bool ffn = h < 27;
  const bool gw = ffn && w < 10, dw = ffn && w >= 10 && w < 15;
  const rsrc_t ro = mk((const char*)wo + (size_t)(h * 128 + 8 * w) * 1280 + s * 160, 8 * 1280);
  const int voff_o = c16 < 10 ? rho * 1280 + c16 * 16 : 0x7fffff00;
  const size_t goff = (size_t)(80 * s + 8 * (gw ? w : 0)) * 3456 + (gw ? h : 0) * 128;
  const rsrc_t rf0 = gw ? mk((const char*)wg + goff, 8 * 3456) : mk((const char*)wd + (size_t)(dw ? 64 * h + 8 * s : 0) * 1280, dw ? 8 * 1280 : 0);
  const rsrc_t rf1 = gw ? mk((const char*)wu + goff, 8 * 3456) : mk((const char*)wd + (size_t)(dw ? 64 * h + 8 * s : 0) * 1280 + 1280, dw ? 7 * 1280 : 0);
  const int voff_f = gw ? (lane >> 3) * 3456 + (lane & 7) * 16 : (lane & 3) * 2560 + ((dw ? w - 10 : 0) * 16 + (lane >> 2)) * 16;
  v4u wot[2], fwt[2];
  const v4u qf = *(LAS v4u*)&lds[L_Q + 4 * c16];
  float sc;
  {
    float xk[16];
#pragma unroll
    for (int i = 0; i < 16; ++i) {
      float d = dot2(kv[i].x, qf.x, 0.f); d = dot2(kv[i].y, qf.y, d); d = dot2(kv[i].z, qf.z, d); d = dot2(kv[i].w, qf.w, d);
      xk[i] = d;
      asm volatile("" : "+v"(xk[i]));
      if (i == 0) wot[0] = ld_nt(ro, voff_o, 0);
      if (i == 1) wot[1] = ld_nt(ro, voff_o, 4 * 1280);
      SCHED();
    }
#pragma unroll
    for (int j = 0; j < 8; ++j) xk[j] = trd<ROR8>(xk[j], xk[j + 8], (lane & 8) != 0);
#pragma unroll
    for (int j = 0; j < 4; ++j) xk[j] = trd<HMIR>(xk[j], xk[j + 4], (lane & 4) != 0);
#pragma unroll
    for (int j = 0; j < 2; ++j) xk[j] = trd<XOR2>(xk[j], xk[j + 2], (lane & 2) != 0);
    const float dot = trd<XOR1>(xk[0], xk[1], (lane & 1) != 0);
    sc = rbf(rbf(dot) * 0.08838834764831845f);
  }
  const float mw = max64(sc);
  const float p = ex2((sc - mw) * LOG2E);
  { const float lw = sum64(p); if (lane == 0) { lds[L_WM + w] = mw; lds[L_WL + w] = lw; } }
  STAMP(6);
  float acc[8];
#pragma unroll
  for (int j = 0; j < 8; ++j) acc[j] = 0.f;
#define PV_ACC(i, reg) { const float pb = dpp<BCAST + (i)>(p); float f[8]; unpack8(reg, f); \
    _Pragma("unroll") for (int j = 0; j < 8; ++j) acc[j] = fmaf(pb, f[j], acc[j]); }
  VM_WAIT(1); fwt[0] = ld_nt(rf0, voff_f, 0); SCHED();
  VM_WAIT(1); fwt[1] = ld_nt(rf1, voff_f, 0); SCHED();
#define PV_LANDED(i) if ((i) < VP) PV_ACC(i, vp[(i) < VP ? (i) : 0]) \
    else if ((i) < VP + VL) { const v4u t_ = *(LAS v4u*)&lds[L_VT + (w * VL + ((i) - VP)) * 256 + lane * 4]; PV_ACC(i, t_) }
  PV_LANDED(0) PV_LANDED(1) PV_LANDED(2) PV_LANDED(3) PV_LANDED(4) PV_LANDED(5) PV_LANDED(6) PV_LANDED(7)
  PV_LANDED(8) PV_LANDED(9) PV_LANDED(10) PV_LANDED(11) PV_LANDED(12) PV_LANDED(13) PV_LANDED(14) PV_LANDED(15)
#undef PV_LANDED
  SCHED();
#pragma unroll
  for (int i = 0; i < VR; ++i) { VM_WAIT(KWIN - 1); kv[i] = ld_nt(rv, lane * 16, toff(VP + VL + i)); SCHED(); }
#define PV_LATE(i) if ((i) >= VP + VL) PV_ACC(i, kv[(i) >= VP + VL ? (i) - VP - VL : 0])
  PV_LATE(0) PV_LATE(1) PV_LATE(2) PV_LATE(3) PV_LATE(4) PV_LATE(5) PV_LATE(6) PV_LATE(7)
  PV_LATE(8) PV_LATE(9) PV_LATE(10) PV_LATE(11) PV_LATE(12) PV_LATE(13) PV_LATE(14) PV_LATE(15)
#undef PV_LATE
#undef PV_ACC
  STAMP(7);
  {
    float y[4];
#pragma unroll
    for (int j = 0; j < 4; ++j) y[j] = tr32(acc[j], acc[j + 4]);
    const float z0 = tr16(y[0], y[2]), z1 = tr16(y[1], y[3]);
    *(LAS v2f*)&lds[L_PART + w * 128 + 8 * c16 + 2 * rho] = v2f{z0, z1};
  }
  BAR();
  STAMP(8);
  const int r8 = lane >> 3;
  float gf[8], uf[8];
  float bmul = 0.f;
  if (w == 15) {
    __builtin_amdgcn_s_setprio(3);
    const int hi = lane >> 5;
    const float wm_a = lds[L_WM + c16], wm_b = lds[L_WM + (c16 & 7) + 8 * hi], wl_a = lds[L_WL + c16];
    v4f pv[8];
#pragma unroll
    for (int k = 0; k < 8; ++k) pv[k] = *(LAS v4f*)&lds[L_PART + (k + 8 * hi) * 128 + 4 * (lane & 31)];
    SCHED();
    const float Mcu = max_row16(wm_a);
    const float fk = ex2((wm_b - Mcu) * LOG2E);
    const float Lcu = sum_row16(ex2((wm_a - Mcu) * LOG2E) * wl_a);
    v4f o4 = {0.f, 0.f, 0.f, 0.f};
#define MRG(k) { const float f = dpp<BCAST + (k)>(fk); o4 += f * pv[k]; }
    MRG(0) MRG(1) MRG(2) MRG(3) MRG(4) MRG(5) MRG(6) MRG(7)
#undef MRG
    st_g(rws, lane * 16, WS_O + (h * 8 + s) * 1280, tr32(o4.x, o4.z), tr32(o4.y, o4.w), tA, tB);
    if (lane == 0) st_g(rws, 64 * 16, WS_O + (h * 8 + s) * 1280, Mcu, Lcu, tA, tB);
    STAMP(9);
    if (D1) __builtin_amdgcn_s_sleep(D1);
    v4u go[8], gml;
    for (unsigned spins = 0; spins < SPIN_CAP; ++spins) {
      asm volatile("" ::: "memory");
#pragma unroll
      for (int k = 0; k < 8; ++k) go[k] = ld_g(rws, lane * 16, WS_O + (h * 8 + k) * 1280);
      gml = ld_g(rws, (lane & 7) * 1280 + 64 * 16, WS_O + h * 8 * 1280);
      unsigned bad = (gml.y ^ tA) | (gml.w ^ tB);
#pragma unroll
      for (int k = 0; k < 8; ++k) bad |= (go[k].y ^ tA) | (go[k].w ^ tB);
      if (__all(bad == 0u)) break;
      __builtin_amdgcn_s_sleep(2);
    }
    STAMP(10);
    const float m = __uint_as_float(gml.x), l = __uint_as_float(gml.z);
    const float M = max_lo8(m);
    const float f = ex2((m - M) * LOG2E);
    const float rL = rcp(sum_lo8(f * l));
    float o0 = 0.f, o1 = 0.f;
#define CMB(k) { const float fb = __uint_as_float(__builtin_amdgcn_readlane(__float_as_uint(f), k)); o0 = fmaf(fb, __uint_as_float(go[k].x), o0); o1 = fmaf(fb, __uint_as_float(go[k].z), o1); }
    CMB(0) CMB(1) CMB(2) CMB(3) CMB(4) CMB(5) CMB(6) CMB(7)
#undef CMB
    *(LAS v2f*)&lds[L_OS + 4 * (lane & 31) + 2 * hi] = v2f{rbf(o0 * rL), rbf(o1 * rL)};
  } else if (gw) {
    unpack8(fwt[0], gf); unpack8(fwt[1], uf);
    bmul = rbf(1.f + lds[L_MOD + 4 * 80 + 8 * w + r8]);
    const float cmul = lds[L_MOD + 3 * 80 + 8 * w + r8];
#pragma unroll
    for (int t = 0; t < 2; ++t) {
      const float mul = t == 0 ? bmul : cmul;
      float y[8];
#pragma unroll
      for (int j = 0; j < 8; ++j) y[j] = tr32(mul * gf[j], mul * uf[j]);
      float z[4];
#pragma unroll
      for (int j = 0; j < 4; ++j) z[j] = tr16(y[j], y[j + 4]);
      const float e0 = trd<ROR8>(z[0], z[2], (lane & 8) != 0), e1 = trd<ROR8>(z[1], z[3], (lane & 8) != 0);
      *(LAS v2f*)&lds[(t == 0 ? L_PB : L_PC) + w * 128 + (lane >> 5) * 64 + 8 * (lane & 7) + 4 * ((lane >> 4) & 1) + 2 * ((lane >> 3) & 1)] = v2f{e0, e1};
    }
  }
  BAR();
  STAMP(11);
  {
    const float o0 = lds[L_OS + 8 * w + rho], o1 = lds[L_OS + 8 * w + 4 + rho];
    float f0[8], f1[8]; unpack8(wot[0], f0); unpack8(wot[1], f1);
    float a[8];
#pragma unroll
    for (int j = 0; j < 8; ++j) a[j] = fmaf(o0, f0[j], o1 * f1[j]);
    float y[4];
#pragma unroll
    for (int j = 0; j < 4; ++j) y[j] = tr32(a[j], a[j + 4]);
    const float z0 = tr16(y[0], y[2]), z1 = tr16(y[1], y[3]);
    if (c16 < 10) *(LAS v2f*)&lds[L_PY + w * 80 + 8 * c16 + 2 * rho] = v2f{z0, z1};
  }
  STAMP(12);
  BAR();
  STAMP(13);
  const int j40 = lane < 40 ? lane : 0;
  if (w == 15) {
    v2f pv[16];
#pragma unroll
    for (int k = 0; k < 16; ++k) pv[k] = *(LAS v2f*)&lds[L_PY + k * 80 + 2 * j40];
    SCHED();
    v2f ya = (pv[0] + pv[1]) + (pv[2] + pv[3]), yb = (pv[4] + pv[5]) + (pv[6] + pv[7]), yc = (pv[8] + pv[9]) + (pv[10] + pv[11]), yd = (pv[12] + pv[13]) + (pv[14] + pv[15]);
    ya = (ya + yb) + (yc + yd);
    const float y0 = ya.x, y1 = ya.y;
    if (lane < 40) st_g(rws, ((lane >> 2) * 32 * 4 + (lane & 3)) * 16, WS_Y + (s * 10 * 32 + h) * 64, y0, y1, tA, tB);
    STAMP(14);
  }
  if (ffn) {
    const int g = h, r = s;
    const int sig = ((lane & 7) >> 1) | ((lane & 1) << 2);
    if (w == 10 || w == 11) {
      const int pb = w == 10 ? L_PB : L_PC;
      float a0 = 0.f, a1 = 0.f;
#pragma unroll
      for (int k = 0; k < 10; ++k) { a0 += lds[pb + k * 128 + lane]; a1 += lds[pb + k * 128 + 64 + lane]; }
      st_g(rws, ((lane >> 3) * 64 + sig) * 16, (w == 10 ? WS_B : WS_C) + (g * 512 + r * 8) * 16, a0, a1, tA, tB);
    }
    float Bg = 0.f, Bu = 0.f, Cg = 0.f, Cu = 0.f;
    if (w == 10) {
      __builtin_amdgcn_s_sleep(16);
      v4u gb[2];
      poll<2, 8>(rws, lane * 16, [&](int k) { return (k == 0 ? WS_B : WS_C) + (g * 8 + r) * 1024; }, true, tA, tB, gb);
      Bg = sum_hi8(__uint_as_float(gb[0].x)); Bu = sum_hi8(__uint_as_float(gb[0].z));
      Cg = sum_hi8(__uint_as_float(gb[1].x)); Cu = sum_hi8(__uint_as_float(gb[1].z));
    }
    if (w < 10) {
      if (D2) __builtin_amdgcn_s_sleep(D2);
      v4u gy[2];
      const unsigned np2 = poll<2, PSL>(rws, c16 * 64 + rho * 16, [&](int k) { return WS_Y + ((r * 10 + w) * 32 + 16 * k) * 64; }, true, tA, tB, gy);
      STAMPV(26, np2);
      STAMP(16);
      const float y0 = sum_row16(__uint_as_float(gy[0].x) + __uint_as_float(gy[1].x));
      const float y1 = sum_row16(__uint_as_float(gy[0].z) + __uint_as_float(gy[1].z));
      const float attn = rbf((lane & 8) ? y1 : y0);
      const int cl = 8 * w + r8;
      const float x1 = rbf(lds[L_XS + cl] + rbf(lds[L_MOD + 2 * 80 + cl] * attn));
      const float xs = x1 * bmul;
      float y[8];
#pragma unroll
      for (int j = 0; j < 8; ++j) y[j] = tr32(xs * gf[j], xs * uf[j]);
      float z[4];
#pragma unroll
      for (int j = 0; j < 4; ++j) z[j] = tr16(y[j], y[j + 4]);
      const float e0 = trd<ROR8>(z[0], z[2], (lane & 8) != 0), e1 = trd<ROR8>(z[1], z[3], (lane & 8) != 0);
      *(LAS v2f*)&lds[L_PA + w * 128 + (lane >> 5) * 64 + 8 * (lane & 7) + 4 * ((lane >> 4) & 1) + 2 * ((lane >> 3) & 1)] = v2f{e0, e1};
      const float mean_w = sum_hi8(x1) * 0.125f;
      const float dd = x1 - mean_w;
      const float m2_w = sum_hi8(dd * dd);
      if (lane == 0) *(LAS v2f*)&lds[L_STAT + 2 * w] = v2f{mean_w, m2_w};
      if ((lane & 7) == 0) lds[L_X1 + cl] = x1;
    }
    STAMP(17);
    BAR();
    STAMP(18);
    if (w == 0) {
      float pa[10], pb_[10];
#pragma unroll
      for (int k = 0; k < 10; ++k) { pa[k] = lds[L_PA + k * 128 + lane]; pb_[k] = lds[L_PA + k * 128 + 64 + lane]; }
      SCHED();
      const float a0 = ((pa[0] + pa[1]) + (pa[2] + pa[3])) + ((pa[4] + pa[5]) + (pa[6] + pa[7])) + (pa[8] + pa[9]);
      const float a1 = ((pb_[0] + pb_[1]) + (pb_[2] + pb_[3])) + ((pb_[4] + pb_[5]) + (pb_[6] + pb_[7])) + (pb_[8] + pb_[9]);
      st_g(rws, ((lane >> 3) * 64 + sig) * 16, WS_A + (g * 512 + r * 8) * 16, a0, a1, tA, tB);
      STAMP(19);
    } else if (w == 1) {
      const v2f sv = *(LAS v2f*)&lds[L_STAT + 2 * (c16 < 10 ? c16 : 0)];
      const float mk_ = c16 < 10 ? sv.x : 0.f;
      const float mean_r = sum_row16(mk_) * 0.1f;
      const float dd = sv.x - mean_r;
      const float m2 = sum_row16(c16 < 10 ? fmaf(8.f * dd, dd, sv.y) : 0.f);
      if (lane == 0) st_g(rws, 0, WS_S + (g * 8 + r) * 128, mean_r, m2, tA, tB);
      STAMP(15);
    } else if (w == 10) {
      if (D3) __builtin_amdgcn_s_sleep(D3);
      v4u ga, gs;
      STAMP(25);
      const unsigned np3 = poll2<1>(rws, lane * 16, WS_A + (g * 8 + r) * 1024, (lane >> 3) * 128, WS_S + g * 1024, tA, tB, ga, gs);
      STAMPV(27, np3);
      STAMP(20);
      const float Ag = sum_hi8(__uint_as_float(ga.x)), Au = sum_hi8(__uint_as_float(ga.z));
      const float mr = __uint_as_float(gs.x);
      const float mean = sum_hi8(mr) * 0.125f;
      const float dd = mr - mean;
      const float var = sum_hi8(fmaf(80.f * dd, dd, __uint_as_float(gs.z))) * (1.f / D);
      const float mb = rbf(mean), den = rbf(__builtin_amdgcn_sqrtf(rbf(rbf(var) + 1e-5f)));
      const float rden = rcp(den);
      const float gate = rbf(fmaf(Ag - mb * Bg, rden, Cg)), up = rbf(fmaf(Au - mb * Bu, rden, Cu));
      const float act = rbf(rbf(silu_fast(gate)) * up);
      if (lane < 8) lds[L_ACT + 2 * (lane & 3) + (lane >> 2)] = act;
    }
    BAR();
    STAMP(21);
    if (dw) {
      const v2f av = *(LAS v2f*)&lds[L_ACT + 2 * (lane & 3)];
      float f0[8], f1[8]; unpack8(fwt[0], f0); unpack8(fwt[1], f1);
      float d[8];
#pragma unroll
      for (int j = 0; j < 8; ++j) d[j] = fmaf(av.x, f0[j], av.y * f1[j]);
      float y[4];
#pragma unroll
      for (int j = 0; j < 4; ++j) y[j] = trd<XOR2>(d[j], d[j + 4], (lane & 2) != 0);
      const float e0 = trd<XOR1>(y[0], y[2], (lane & 1) != 0), e1 = trd<XOR1>(y[1], y[3], (lane & 1) != 0);
      st_g(rws, lane * 16, WS_D + ((g * 8 + r) * 320 + (w - 10) * 64) * 16, e0, e1, tA, tB);
      STAMP(22);
    }
    if (g < 5) {
      if (D4) __builtin_amdgcn_s_sleep(D4);
      v4u gd[2];
      const int n1 = w + 16 < 27 ? w + 16 : w;
      const int voff = (lane >> 3) * 5120 + (lane & 7) * 16;
      STAMP(23);
      const unsigned np4 = poll<2, 1>(rws, voff, [&](int k) { return WS_D + (k == 0 ? w : n1) * 8 * 5120 + (40 * r + 8 * g) * 16; }, true, tA, tB, gd);
      STAMPV(28, np4);
      STAMP(24);
      float e0 = __uint_as_float(gd[0].x), e1 = __uint_as_float(gd[0].z);
      if (w + 16 < 27) { e0 += __uint_as_float(gd[1].x); e1 += __uint_as_float(gd[1].z); }
      e0 = sum_hi8(e0); e1 = sum_hi8(e1);
      if (lane < 8) *(LAS v2f*)&lds[L_PD + w * 16 + 2 * lane] = v2f{e0, e1};
      BAR();
      if (w == 0 && lane < 8) {
        v2f pv[16];
#pragma unroll
        for (int k = 0; k < 16; ++k) pv[k] = *(LAS v2f*)&lds[L_PD + k * 16 + 2 * lane];
        const int cl = 16 * g + 2 * lane;
        const v2f x1v = *(LAS v2f*)&lds[L_X1 + cl], gbv = *(LAS v2f*)&lds[L_MOD + 5 * 80 + cl];
        SCHED();
        v2f ya = (pv[0] + pv[1]) + (pv[2] + pv[3]), yb = (pv[4] + pv[5]) + (pv[6] + pv[7]), yc = (pv[8] + pv[9]) + (pv[10] + pv[11]), yd = (pv[12] + pv[13]) + (pv[14] + pv[15]);
        ya = (ya + yb) + (yc + yd);
        const float f0 = ya.x, f1 = ya.y;
        const float o0 = x1v.x + rbf(gbv.x * rbf(f0));
        const float o1 = x1v.y + rbf(gbv.y * rbf(f1));
        ((unsigned*)out)[(80 * r + cl) >> 1] = bfbits(o0) | (bfbits(o1) << 16);
        if (b == 0 && lane == 0) *(uint2*)(ws + WS_EPOCH) = make_uint2(tA, tB);
      }
    }
  }
  STAMP(30);
}

template <int KWIN, int VP, int VL, int PSL, int D1, int D2, int D3, int D4, int TOUCH>
static void launch(void* const* d_in, void* d_out, void* d_ws, hipStream_t stream) {
  adaln_decode_kernel<KWIN, VP, VL, PSL, D1, D2, D3, D4, TOUCH><<<256, 1024, 0, stream>>>(
      (const u16*)d_in[0], (const u16*)d_in[1], (const u16*)d_in[2], (const u16*)d_in[3], (const u16*)d_in[4], (const u16*)d_in[6],
      (const u16*)d_in[7], (const u16*)d_in[8], (const u16*)d_in[9], (const u16*)d_in[10], (u16*)d_out, (unsigned char*)d_ws);
}

}

extern "C" void kernel_launch(void* const* d_in, const int* in_sizes, int n_in, void* d_out, int out_size, void* d_ws, size_t ws_size,
                              hipStream_t stream) {
  adaln::launch<2, 6, 6, 8, 8, 8, 8, 8, 1>(d_in, d_out, d_ws, stream);
}
```

```cpp
#include <hip/hip_runtime.h>
#include <stdint.h>

#define DBG_STAMPS 1
#define POL_K 2
#define POL_V 2
#define POL_W 2

namespace adaln {

typedef unsigned short u16;
typedef unsigned long long u64;
typedef unsigned v4u __attribute__((ext_vector_type(4)));
typedef float v2f __attribute__((ext_vector_type(2)));
typedef float v4f __attribute__((ext_vector_type(4)));
typedef __bf16 bf16x2 __attribute__((ext_vector_type(2)));
typedef __bf16 bf16x8 __attribute__((ext_vector_type(8)));
#define LAS __attribute__((address_space(3)))

constexpr int D = 640, SK = 8192, F = 1728;
constexpr int AUX_NT = 2, AUX_SC1 = 16;
constexpr int RSRC3 = 0x00020000;
constexpr float LOG2E = 1.4426950408889634f;
constexpr unsigned SPIN_CAP = 1u << 16;

constexpr int WS_EPOCH = 0;
constexpr int WS_T0 = 128;
constexpr int WS_MOD = 256;
constexpr int WS_Q = WS_MOD + 4 * 1920 * 16;
constexpr int WS_O = WS_Q + 32 * 8 * 64 * 16;
constexpr int WS_Y = WS_O + 32 * 8 * 640;
constexpr int WS_A = WS_Y + 8 * 10 * 32 * 2 * 16;
constexpr int WS_BC = WS_A + 216 * 8 * 128;
constexpr int WS_X1 = WS_BC + 216 * 8 * 128;
constexpr int WS_D = WS_X1 + 160 * 16;
constexpr int WS_END = WS_D + 20 * 24 * 128;
constexpr int WS_DBG = 4 << 20;

constexpr int L_Q = 0;
constexpr int L_FLAG = L_Q + 64;
constexpr int L_HS = L_FLAG + 4;
constexpr int L_SC = L_HS + 80;
constexpr int L_TMP = L_SC + 192;
constexpr int L_MOD = L_TMP + 64;
constexpr int L_WM = L_MOD + 480;
constexpr int L_WL = L_WM + 16;
constexpr int L_STAT = L_WL + 16;
constexpr int L_X1 = L_STAT + 32;
constexpr int L_XS = L_X1 + 80;
constexpr int L_OS = L_XS + 80;
constexpr int L_GB = L_OS + 128;
constexpr int L_PD = L_GB + 32;
constexpr int L_PART = L_PD + 576;
constexpr int L_PY = L_PART + 2112;
constexpr int L_PA = L_PY + 1344;
constexpr int L_PB = L_PA + 1280;
constexpr int L_PC = L_PB + 1280;
constexpr int L_VT = L_PC + 1280;
static_assert(L_VT % 4 == 0 && L_PART % 4 == 0 && L_VT - L_PART >= 5120, "alignment; room for the wq tile");

__device__ __forceinline__ float bflo(unsigned u) { return __uint_as_float(u << 16); }
__device__ __forceinline__ float bfhi(unsigned u) { return __uint_as_float(u & 0xffff0000u); }
__device__ __forceinline__ float rbf(float f) { float r = (float)(__bf16)f; asm("" : "+v"(r)); return r; }
__device__ __forceinline__ unsigned bfbits(float f) { return (unsigned)__builtin_bit_cast(u16, (__bf16)f); }
__device__ __forceinline__ float ex2(float x) { return __builtin_amdgcn_exp2f(x); }
__device__ __forceinline__ float silu(float x) { return x / (1.f + ex2(-x * LOG2E)); }
__device__ __forceinline__ float rcp(float x) { return __builtin_amdgcn_rcpf(x); }
__device__ __forceinline__ float silu_fast(float x) { return x * rcp(1.f + ex2(-x * LOG2E)); }
__device__ __forceinline__ float dot2(unsigned a, unsigned b, float acc) {
  return __builtin_amdgcn_fdot2_f32_bf16(__builtin_bit_cast(bf16x2, a), __builtin_bit_cast(bf16x2, b), acc, false);
}
__device__ __forceinline__ v4f mfma16(const v4u& a, const v4u& b, const v4f& c) {
  return __builtin_amdgcn_mfma_f32_16x16x32_bf16(__builtin_bit_cast(bf16x8, a), __builtin_bit_cast(bf16x8, b), c, 0, 0, 0);
}
__device__ __forceinline__ unsigned bfpair(float exact_bf16) { const unsigned u = __float_as_uint(exact_bf16); return (u >> 16) | (u & 0xffff0000u); }
__device__ __forceinline__ v4u vand(unsigned a, const v4u& m) { return v4u{a & m.x, a & m.y, a & m.z, a & m.w}; }
template <int CTRL> __device__ __forceinline__ unsigned dppu(unsigned x) { return (unsigned)__builtin_amdgcn_mov_dpp((int)x, CTRL, 0xf, 0xf, true); }
template <int CTRL> __device__ __forceinline__ float dpp(float x) {
  return __builtin_bit_cast(float, __builtin_amdgcn_mov_dpp(__builtin_bit_cast(int, x), CTRL, 0xf, 0xf, true));
}
constexpr int XOR1 = 0xB1, XOR2 = 0x4E, HMIR = 0x141, MIR = 0x140, ROR4 = 0x124, ROR8 = 0x128, BCAST = 0x150;
__device__ __forceinline__ float tr32(float a, float b) {
  auto r = __builtin_amdgcn_permlane32_swap(__float_as_uint(a), __float_as_uint(b), false, false);
  return __uint_as_float(r[0]) + __uint_as_float(r[1]);
}
__device__ __forceinline__ float tr16(float a, float b) {
  auto r = __builtin_amdgcn_permlane16_swap(__float_as_uint(a), __float_as_uint(b), false, false);
  return __uint_as_float(r[0]) + __uint_as_float(r[1]);
}
__device__ __forceinline__ float max32(float a) {
  auto r = __builtin_amdgcn_permlane32_swap(__float_as_uint(a), __float_as_uint(a), false, false);
  return fmaxf(__uint_as_float(r[0]), __uint_as_float(r[1]));
}
__device__ __forceinline__ float max16(float a) {
  auto r = __builtin_amdgcn_permlane16_swap(__float_as_uint(a), __float_as_uint(a), false, false);
  return fmaxf(__uint_as_float(r[0]), __uint_as_float(r[1]));
}
template <int CTRL> __device__ __forceinline__ float trd(float a, float b, bool hi) {
  const float t1 = a + dpp<CTRL>(a), t2 = b + dpp<CTRL>(b);
  return hi ? t2 : t1;
}
__device__ __forceinline__ float sum_row16(float x) { x += dpp<XOR1>(x); x += dpp<XOR2>(x); x += dpp<HMIR>(x); x += dpp<MIR>(x); return x; }
__device__ __forceinline__ float max_row16(float x) { x = fmaxf(x, dpp<XOR1>(x)); x = fmaxf(x, dpp<XOR2>(x)); x = fmaxf(x, dpp<HMIR>(x)); return fmaxf(x, dpp<MIR>(x)); }
__device__ __forceinline__ float sum_lo8(float x) { x += dpp<XOR1>(x); x += dpp<XOR2>(x); x += dpp<HMIR>(x); return x; }
__device__ __forceinline__ float max_lo8(float x) { x = fmaxf(x, dpp<XOR1>(x)); x = fmaxf(x, dpp<XOR2>(x)); x = fmaxf(x, dpp<HMIR>(x)); return x; }
__device__ __forceinline__ float sum_hi8(float x) { x += dpp<ROR8>(x); x = tr16(x, x); x = tr32(x, x); return x; }
__device__ __forceinline__ float sum64(float x) { x = sum_row16(x); x = tr16(x, x); return tr32(x, x); }
__device__ __forceinline__ float max64(float x) { return max32(max16(max_row16(x))); }
__device__ __forceinline__ v4f fold2(const v4f& a, const v4f& b) {
  v4f t;
#pragma unroll
  for (int i = 0; i < 4; ++i) {
    auto r = __builtin_amdgcn_permlane32_swap(__float_as_uint(a[i]), __float_as_uint(b[i]), false, false);
    t[i] = __uint_as_float(r[0]) + dpp<0x128>(__uint_as_float(r[1]));
  }
  return t;
}
__device__ __forceinline__ void unpack8(const v4u& w, float (&f)[8]) {
  f[0] = bflo(w.x); f[1] = bfhi(w.x); f[2] = bflo(w.y); f[3] = bfhi(w.y); f[4] = bflo(w.z); f[5] = bfhi(w.z); f[6] = bflo(w.w); f[7] = bfhi(w.w);
}

typedef __amdgpu_buffer_rsrc_t rsrc_t;
__device__ __forceinline__ rsrc_t mk(const void* p, unsigned bytes) { return __builtin_amdgcn_make_buffer_rsrc((void*)p, 0, (int)bytes, RSRC3); }
template <int POL> __device__ __forceinline__ v4u ld_p(rsrc_t r, int voff, int soff) { return __builtin_amdgcn_raw_buffer_load_b128(r, voff, soff, POL); }
#define ld_k ld_p<POL_K>
#define ld_v ld_p<POL_V>
#define ld_w ld_p<POL_W>
__device__ __forceinline__ v4u ld_g(rsrc_t r, int voff, int soff) { return __builtin_amdgcn_raw_buffer_load_b128(r, voff, soff, AUX_SC1); }
__device__ __forceinline__ void st_g(rsrc_t r, int voff, int soff, float a, float b, unsigned tA, unsigned tB) {
  v4u v = {__float_as_uint(a), tA, __float_as_uint(b), tB};
  __builtin_amdgcn_raw_buffer_store_b128(v, r, voff, soff, AUX_SC1);
}
struct Tag4 { unsigned a, b, c, d; };
__device__ __forceinline__ unsigned pk(float v, unsigned t) { return ((__float_as_uint(v) + 0x800u) & 0xfffff000u) | t; }
__device__ __forceinline__ float upk(unsigned u) { return __uint_as_float(u & 0xfffff000u); }
__device__ __forceinline__ void st_p(rsrc_t r, int voff, int soff, float a, float b, float c, float d, const Tag4& t) {
  v4u v = {pk(a, t.a), pk(b, t.b), pk(c, t.c), pk(d, t.d)};
  __builtin_amdgcn_raw_buffer_store_b128(v, r, voff, soff, AUX_SC1);
}
__device__ __forceinline__ unsigned bad_p(const v4u& g, const Tag4& t) { return ((g.x ^ t.a) | (g.y ^ t.b) | (g.z ^ t.c) | (g.w ^ t.d)) & 0xfffu; }
template <int N, int SLP, typename VOFF, typename SOFF>
__device__ __forceinline__ unsigned pollp(rsrc_t rs, VOFF voff, SOFF soff, bool active, const Tag4& t, v4u (&g)[N]) {
  unsigned spins = 0;
  for (; spins < SPIN_CAP; ++spins) {
    asm volatile("" ::: "memory");
#pragma unroll
    for (int k = 0; k < N; ++k) g[k] = __builtin_amdgcn_raw_buffer_load_b128(rs, voff(k), soff(k), AUX_SC1);
    unsigned bad = 0;
#pragma unroll
    for (int k = 0; k < N; ++k) bad |= bad_p(g[k], t);
    if (__all(bad == 0u || !active)) break;
    if (SLP) __builtin_amdgcn_s_sleep(SLP);
  }
  return spins;
}
__device__ __forceinline__ bool tag_ok(const v4u& g, unsigned tA, unsigned tB) { return g.y == tA && g.w == tB; }
template <int POL> __device__ __forceinline__ void glds(const void* gsrc, unsigned lds_dst) {
  unsigned keep;
  if (POL == 2)
    asm volatile("s_mov_b32 %0, m0\n\ts_mov_b32 m0, %2\n\ts_nop 0\n\tglobal_load_lds_dwordx4 %1, off nt\n\ts_mov_b32 m0, %0"
                 : "=&s"(keep) : "v"(gsrc), "s"(lds_dst) : "memory");
  else
    asm volatile("s_mov_b32 %0, m0\n\ts_mov_b32 m0, %2\n\ts_nop 0\n\tglobal_load_lds_dwordx4 %1, off\n\ts_mov_b32 m0, %0"
                 : "=&s"(keep) : "v"(gsrc), "s"(lds_dst) : "memory");
}
#define LDS_WAIT() asm volatile("s_waitcnt lgkmcnt(0)" ::: "memory")
#define VM_WAIT(n) asm volatile("s_waitcnt vmcnt(%0)" :: "i"(n) : "memory")
#define BAR() do { asm volatile("s_waitcnt lgkmcnt(0)" ::: "memory"); __builtin_amdgcn_s_barrier(); asm volatile("" ::: "memory"); } while (0)
#define SCHED() __builtin_amdgcn_sched_barrier(0)

template <int N, int SLP, typename OFF>
__device__ __forceinline__ unsigned poll(rsrc_t rs, int voff, OFF soff, bool active, unsigned tA, unsigned tB, v4u (&g)[N]) {
  unsigned spins = 0;
  for (; spins < SPIN_CAP; ++spins) {
    asm volatile("" ::: "memory");
    bool ok = true;
#pragma unroll
    for (int k = 0; k < N; ++k) g[k] = ld_g(rs, voff, soff(k));
#pragma unroll
    for (int k = 0; k < N; ++k) ok = ok && tag_ok(g[k], tA, tB);
    if (__all(ok || !active)) break;
    if (SLP) __builtin_amdgcn_s_sleep(SLP);
  }
  return spins;
}
template <int SLP>
__device__ __forceinline__ unsigned poll2(rsrc_t rs, int voff0, int soff0, int voff1, int soff1, unsigned tA, unsigned tB, v4u& g0, v4u& g1) {
  unsigned spins = 0;
  for (; spins < SPIN_CAP; ++spins) {
    asm volatile("" ::: "memory");
    g0 = ld_g(rs, voff0, soff0);
    g1 = ld_g(rs, voff1, soff1);
    if (__all(tag_ok(g0, tA, tB) && tag_ok(g1, tA, tB))) break;
    if (SLP) __builtin_amdgcn_s_sleep(SLP);
  }
  return spins;
}

__device__ __forceinline__ u64 rt() { return __builtin_amdgcn_s_memrealtime(); }
#define STAMPV(n, v) do { if (lane == (n)) tsv = (u64)(v) + 1; } while (0)
#define STAMP(n) do { if ((n) == 0 || (n) == 0) { const u64 t_ = rt(); if (lane == (n)) tsv = t_; } } while (0)

template <typename... A> constexpr int uid_of(A... a) { unsigned long long z = 7; for (int v : {a...}) z = (z * 1000003ull + (unsigned)v) % 999999937ull; return (int)z; }
template <int KWIN, int VP, int VL, int PSL, int D1, int D2, int D3, int D4, int TOUCH, int DT, int T0S, int TM, int DQ, int CPF, int CTB, int KW2, int X1, int X2, int X3, int X4, int TLBT, int BURST, int DM, int DY>
__global__ void __launch_bounds__(1024, 4)
adaln_decode_kernel(const u16* __restrict__ x, const u16* __restrict__ Kc, const u16* __restrict__ Vc, const u16* __restrict__ wq,
                    const u16* __restrict__ wo, const u16* __restrict__ wg, const u16* __restrict__ wu, const u16* __restrict__ wd,
                    const u16* __restrict__ cnd, const u16* __restrict__ wmod, u16* __restrict__ out, unsigned char* __restrict__ ws) {
  constexpr int UID = uid_of(KWIN, VP, VL, PSL, D1, D2, D3, D4, TOUCH, DT, T0S, TM, DQ, CPF, CTB, KW2, X1, X2, X3, X4, TLBT, BURST, DM, DY);
  constexpr int VR = 16 - VP - VL;
  static_assert(VR >= 2 && VR <= 12, "");
  __shared__ __attribute__((aligned(16))) float lds[L_VT + 16 * (VL > 0 ? VL : 1) * 256];
  const int tid = threadIdx.x, lane = tid & 63, w = __builtin_amdgcn_readfirstlane(tid >> 6);
  const int b = blockIdx.x;
  const int h = (b & 7) * 4 + (b >> 6), s = (b >> 3) & 7;
  const int rho = lane >> 4, c16 = lane & 15;
  u64 tsv = 0;
  STAMP(0);
  asm volatile("" :: "s"(x), "s"(Kc), "s"(Vc), "s"(wq), "s"(cnd), "s"(wmod), "s"(ws));
  const u64 clk0 = __builtin_amdgcn_s_memtime();
  volatile LAS unsigned* qflag = (volatile LAS unsigned*)&lds[L_FLAG];
  if (tid == 0) *qflag = 0u;
  const rsrc_t rws = mk(ws, WS_END);
  const size_t kvbase = ((size_t)h * SK + s * 1024) * 256;
  const rsrc_t rk = mk((const char*)Kc + kvbase, 262144);
  const rsrc_t rv = mk((const char*)Vc + kvbase, 262144);
  const int rot = ((b >> 3) * 5 + (b & 7) * 3) & 15;
  auto toff = [&](int i) { return ((((i + rot) & 15) << 4) + w) << 10; };
  typedef unsigned v2u __attribute__((ext_vector_type(2)));
  const v2u ep = __builtin_amdgcn_raw_buffer_load_b64(rws, 0, WS_EPOCH, 0);
  unsigned tA, tB; Tag4 tg;
  auto get_tags = [&]() {
    tA = __builtin_amdgcn_readfirstlane(ep.x) + 1u; tB = __builtin_amdgcn_readfirstlane(ep.y) + 1u;
    u64 z = (((u64)tB << 32) | tA) * 0x9E3779B97F4A7C15ull; z ^= z >> 29; z *= 0xBF58476D1CE4E5B9ull; z ^= z >> 32;
    tg.a = (unsigned)z & 0xfffu; tg.b = (unsigned)(z >> 12) & 0xfffu; tg.c = (unsigned)(z >> 24) & 0xfffu; tg.d = (unsigned)(z >> 36) & 0xfffu;
  };

  const unsigned tbase = (unsigned)rt();
  volatile LAS unsigned* tb_lds = (volatile LAS unsigned*)&lds[L_FLAG + 1];
  if (tid == 0) *tb_lds = tbase;
  auto pace = [&](int n) {
    VM_WAIT(KWIN - 1);
    if (DT) { const unsigned due = (unsigned)(T0S + n * DT / 16), tb = *tb_lds; while ((int)((unsigned)rt() - tb - due) < 0) __builtin_amdgcn_s_sleep(1); }
  };
  auto paceq = [&](int n) {
    VM_WAIT(KW2 - 1);
    if (DT) { const unsigned due = (unsigned)(T0S + n * DT / 16), tb = *tb_lds; while ((int)((unsigned)rt() - tb - due) < 0) __builtin_amdgcn_s_sleep(1); }
  };
  auto not_before = [&](int xt) {
    if (xt) { const unsigned due = (unsigned)(T0S + (16 + VP + VL + VR + 1) * DT / 16 + xt), tb = *tb_lds; while ((int)((unsigned)rt() - tb - due) < 0) __builtin_amdgcn_s_sleep(1); }
  };
  v4u kv[16], vp[VP > 0 ? VP : 1];
  const unsigned vt_lds = (unsigned)(uintptr_t)(LAS float*)&lds[L_VT] + (unsigned)w * (VL * 1024);
  const char* vgl = (const char*)Vc + kvbase + lane * 16;
  const int cb = b % 60, rq = b / 60;
  const bool has_mod = b < 240;
  const int r8 = lane >> 3, hi = lane >> 5;
  unsigned pf0 = 0, pf1 = 0, pf2 = 0, pf3 = 0;
  v4u mt[2]; u16 cv[2];
  unsigned xw[5], xs2 = 0;
  if (w < 10) {
    const int r0 = (has_mod ? rq : 0) * 160 + 16 * w;
    const rsrc_t rm = mk((const char*)wmod + (size_t)r0 * 7680 + (has_mod ? cb : 0) * 128, has_mod ? 16 * 7680 : 0);
    mt[0] = ld_w(rm, r8 * 7680 + (lane & 7) * 16, 0); mt[1] = ld_w(rm, r8 * 7680 + (lane & 7) * 16, 8 * 7680);
    cv[0] = cnd[r0 + r8]; cv[1] = cnd[r0 + 8 + r8];
    if (w == 0) {
#pragma unroll
      for (int k = 0; k < 5; ++k) xw[k] = ((const unsigned*)x)[lane + 64 * k];
      xs2 = ((const unsigned*)x)[40 * s + (lane < 40 ? lane : 0)];
    }
  } else if (w < 15) {
    const char* gq = (const char*)wq + (size_t)(s * 80 + 16 * (w - 10) + rho) * 8192 + h * 256 + c16 * 16;
    const unsigned dst = (unsigned)(uintptr_t)(LAS float*)&lds[L_PART] + (w - 10) * 4096;
#pragma unroll
    for (int u = 0; u < 4; ++u) glds<POL_W>(gq + (size_t)u * 4 * 8192, dst + u * 1024);
  } else {
    if (CPF) {
      u64 pc; unsigned len;
      asm volatile("s_getpc_b64 %0\n.Lpc%=:\n\ts_mov_b32 %1, adaln_end_%c2-.Lpc%=" : "=s"(pc), "=s"(len) : "i"(UID));
      const unsigned off = (unsigned)(b >> 3) * 1024 + (lane & 7) * 64;
      const rsrc_t rc = mk((const void*)pc, len);
      pf0 = __builtin_amdgcn_raw_buffer_load_b32(rc, off, 0, 0); pf1 = __builtin_amdgcn_raw_buffer_load_b32(rc, off ^ 512, 0, 0);
    }
    if (TLBT) {
      const char* pt = lane == 0 ? (const char*)Vc + kvbase : lane == 1 ? (const char*)wo + (size_t)(h * 128) * 1280 : lane == 2 ? (const char*)wg + (size_t)(80 * s) * 3456
                     : lane == 3 ? (const char*)wu + (size_t)(80 * s) * 3456 : (const char*)wd + (size_t)(72 * (b < 240 ? b / 10 : 0)) * 1280;
      pf3 = __builtin_nontemporal_load((const unsigned*)pt);
    }
    if (TOUCH) {
      const int g_ = h < 27 ? h : 0;
      int o0;
      if (lane < 5) o0 = WS_O + (h * 8 + s) * 640 + lane * 128;
      else if (lane < 15) o0 = WS_Y + ((s * 10 + (lane - 5)) * 32 + h) * 32;
      else if (lane < 23) o0 = WS_A + ((8 * g_ + (lane - 15)) * 8 + s) * 128;
      else if (lane < 31) o0 = WS_BC + ((8 * g_ + (lane - 23)) * 8 + s) * 128;
      else if (lane < 33) o0 = WS_D + (((b % 10) * 2 + (lane - 31)) * 24 + (b < 240 ? b / 10 : 0)) * 128;
      else o0 = WS_X1 + s * 320;
      pf2 = __builtin_amdgcn_raw_buffer_load_b32(rws, o0, 0, TOUCH == 1 ? AUX_SC1 : 0);
    }
  }
  SCHED();
  if (w != 0) { if (DT) { while ((unsigned)rt() - tbase < (unsigned)T0S) __builtin_amdgcn_s_sleep(1); } kv[0] = ld_k(rk, lane * 16, toff(0)); SCHED(); }
  if (w < 10) {
    const float s0 = rbf(silu(bflo(cv[0]))), s1 = rbf(silu(bflo(cv[1])));
    float f0[8], f1[8]; unpack8(mt[0], f0); unpack8(mt[1], f1);
    float y[4];
#pragma unroll
    for (int j = 0; j < 4; ++j) y[j] = tr32(fmaf(s0, f0[j], s1 * f1[j]), fmaf(s0, f0[j + 4], s1 * f1[j + 4]));
    float z0 = tr16(y[0], y[2]), z1 = tr16(y[1], y[3]);
    z0 += dpp<ROR8>(z0); z1 += dpp<ROR8>(z1);
    if ((lane & 8) == 0) *(LAS v2f*)&lds[L_PC + w * 64 + 8 * (lane & 7) + 2 * rho] = v2f{z0, z1};
  } else if (w < 15) {
    VM_WAIT(1);
  }
  STAMP(15);
  BAR();
  STAMP(32);
  SCHED();
  get_tags();
  STAMP(33);
  if (w == 0) {
    {
      v2f pv[10];
#pragma unroll
      for (int k = 0; k < 10; ++k) pv[k] = *(LAS v2f*)&lds[L_PC + k * 64 + 2 * (lane & 31)];
      const v2f v = (((pv[0] + pv[1]) + (pv[2] + pv[3])) + ((pv[4] + pv[5]) + (pv[6] + pv[7]))) + (pv[8] + pv[9]);
      asm volatile("" :: "v"(v.x), "v"(v.y));
      STAMP(34);
      if (has_mod && lane < 32) st_g(rws, lane * 16, WS_MOD + (rq * 1920 + cb * 32) * 16, v.x, v.y, tA, tB);
      if (b == 0 && lane == 0) st_g(rws, 0, WS_T0, __uint_as_float(tbase), 0.f, tA, tB);
    }
    STAMP(1);
    float mean_b, den_b;
    {
      float xv[10];
#pragma unroll
      for (int k = 0; k < 5; ++k) { xv[2 * k] = bflo(xw[k]); xv[2 * k + 1] = bfhi(xw[k]); }
      float sm = 0.f;
#pragma unroll
      for (int k = 0; k < 10; ++k) sm += xv[k];
      const float mean = sum64(sm) * (1.f / D);
      float vs = 0.f;
#pragma unroll
      for (int k = 0; k < 10; ++k) { const float d = xv[k] - mean; vs = fmaf(d, d, vs); }
      const float var = sum64(vs) * (1.f / D);
      mean_b = rbf(mean); den_b = rbf(sqrtf(rbf(rbf(var) + 1e-5f)));
    }
    const int j40 = lane < 40 ? lane : 0;
    STAMP(35);
    if (lane < 40) *(LAS v2f*)&lds[L_XS + 2 * lane] = v2f{bflo(xs2), bfhi(xs2)};
    {
      v4u g[8];
      while ((unsigned)rt() - tbase < (unsigned)TM) __builtin_amdgcn_s_sleep(2);
      STAMP(36);
      const unsigned npm = poll<8, PSL>(rws, j40 * 16, [&](int k) { return WS_MOD + ((k & 3) * 1920 + (k >> 2) * 320 + 40 * s) * 16; }, lane < 40, tA, tB, g);
      STAMPV(40, npm);
      STAMP(37);
      const float sa0 = rbf((__uint_as_float(g[0].x) + __uint_as_float(g[1].x)) + (__uint_as_float(g[2].x) + __uint_as_float(g[3].x)));
      const float sa1 = rbf((__uint_as_float(g[0].z) + __uint_as_float(g[1].z)) + (__uint_as_float(g[2].z) + __uint_as_float(g[3].z)));
      const float ca0 = rbf((__uint_as_float(g[4].x) + __uint_as_float(g[5].x)) + (__uint_as_float(g[6].x) + __uint_as_float(g[7].x)));
      const float ca1 = rbf((__uint_as_float(g[4].z) + __uint_as_float(g[5].z)) + (__uint_as_float(g[6].z) + __uint_as_float(g[7].z)));
      const float h0 = rbf(rbf(rbf(rbf(bflo(xs2) - mean_b) / den_b) * rbf(1.f + ca0)) + sa0);
      const float h1 = rbf(rbf(rbf(rbf(bfhi(xs2) - mean_b) / den_b) * rbf(1.f + ca1)) + sa1);
      if (lane < 40) *(LAS v2f*)&lds[L_HS + 2 * lane] = v2f{h0, h1};
      VM_WAIT(0);
      LDS_WAIT();
    }
    STAMP(2);
    {
      float acc[8];
#pragma unroll
      for (int j = 0; j < 8; ++j) acc[j] = 0.f;
#pragma unroll
      for (int u = 0; u < 20; ++u) {
        const float hv = lds[L_HS + 4 * u + rho];
        const v4u tq = *(LAS v4u*)&lds[L_PART + u * 256 + lane * 4];
        float f[8]; unpack8(tq, f);
#pragma unroll
        for (int j = 0; j < 8; ++j) acc[j] = fmaf(hv, f[j], acc[j]);
      }
      float y[4];
#pragma unroll
      for (int j = 0; j < 4; ++j) y[j] = tr32(acc[j], acc[j + 4]);
      const float z0 = tr16(y[0], y[2]), z1 = tr16(y[1], y[3]);
      st_g(rws, lane * 16, WS_Q + (h * 8 + s) * 1024, z0, z1, tA, tB);
    }
    STAMP(3);
    {
      v4u g[8];
      if (CTB) {
        const v4u gt = ld_g(rws, 0, WS_T0);
        if (tag_ok(gt, tA, tB) && (int)(gt.x - tbase) < 0 && (int)(tbase - gt.x) < 400 && lane == 0) *tb_lds = gt.x;
      } else if (DQ) __builtin_amdgcn_s_sleep(DQ);
      STAMP(38);
      const unsigned npq = poll<8, 4>(rws, lane * 16, [&](int k) { return WS_Q + (h * 8 + k) * 1024; }, true, tA, tB, g);
      STAMPV(41, npq);
      float q0 = 0.f, q1 = 0.f;
#pragma unroll
      for (int k = 0; k < 8; ++k) { q0 += __uint_as_float(g[k].x); q1 += __uint_as_float(g[k].z); }
      ((LAS unsigned*)lds)[L_Q + 4 * c16 + rho] = bfbits(q0) | (bfbits(q1) << 16);
    }
    LDS_WAIT();
    *qflag = 1u;
    STAMP(4);
    STAMP(39);
#pragma unroll
    for (int i = 0; i < 16; ++i) kv[i] = ld_k(rk, lane * 16, toff(i));
    SCHED();
#pragma unroll
    for (int i = 0; i < VP; ++i) vp[i] = ld_v(rv, lane * 16, toff(i));
#pragma unroll
    for (int i = 0; i < VL; ++i) glds<POL_V>(vgl + toff(VP + i), vt_lds + i * 1024);
    SCHED();
#pragma unroll
    for (int ch = 2; ch < 6; ++ch) {
      v4u g[4];
      poll<4, 1>(rws, j40 * 16, [&](int k) { return WS_MOD + (k * 1920 + ch * 320 + 40 * s) * 16; }, lane < 40, tA, tB, g);
      const float m0 = rbf((__uint_as_float(g[0].x) + __uint_as_float(g[1].x)) + (__uint_as_float(g[2].x) + __uint_as_float(g[3].x)));
      const float m1 = rbf((__uint_as_float(g[0].z) + __uint_as_float(g[1].z)) + (__uint_as_float(g[2].z) + __uint_as_float(g[3].z)));
      if (lane < 40) *(LAS v2f*)&lds[L_MOD + ch * 80 + 2 * lane] = v2f{m0, m1};
    }
    VM_WAIT(0);
  } else {
#pragma unroll
    for (int i = 1; i < 16; ++i) { pace(i); kv[i] = ld_k(rk, lane * 16, toff(i)); SCHED(); }
#pragma unroll
    for (int i = 0; i < VP; ++i) { pace(16 + i); vp[i] = ld_v(rv, lane * 16, toff(i)); SCHED(); }
#pragma unroll
    for (int i = 0; i < VL; ++i) { pace(16 + VP + i); glds<POL_V>(vgl + toff(VP + i), vt_lds + i * 1024); SCHED(); }
    asm volatile("" :: "v"(pf0), "v"(pf1), "v"(pf2), "v"(pf3));
    STAMP(4);
    for (unsigned spins = 0; spins < (SPIN_CAP << 6); ++spins) { if (*qflag != 0u) break; __builtin_amdgcn_s_sleep(1); }
    asm volatile("" ::: "memory");
  }
  STAMP(5);
  const bool ffn = h < 27, gw = w < 10;
  const rsrc_t ro = mk((const char*)wo + (size_t)(h * 128 + 8 * w) * 1280 + s * 160, 8 * 1280);
  const int voff_o = c16 < 10 ? rho * 1280 + c16 * 16 : 0x7fffff00;
  const size_t goff = (size_t)(80 * s + 8 * (gw ? w : 0)) * 3456 + (ffn ? h : 0) * 128;
  const rsrc_t rg = mk((const char*)wg + goff, (ffn && gw) ? 8 * 3456 : 0), ru = mk((const char*)wu + goff, (ffn && gw) ? 8 * 3456 : 0);
  const int drho = b < 240 ? b / 10 : 0, dkap = b % 10;
  const bool dw = b < 240 && w < 9;
  const rsrc_t rd = mk((const char*)wd + (size_t)(72 * drho + 8 * (w < 9 ? w : 0)) * 1280 + dkap * 128, dw ? 8 * 1280 : 0);
  v4u wot[2], gut[2], dnt;
  const v4u qf = *(LAS v4u*)&lds[L_Q + 4 * c16];
  float sc;
  {
    float xk[16];
#pragma unroll
    for (int i = 0; i < 16; ++i) {
      float d = dot2(kv[i].x, qf.x, 0.f); d = dot2(kv[i].y, qf.y, d); d = dot2(kv[i].z, qf.z, d); d = dot2(kv[i].w, qf.w, d);
      xk[i] = d;
      asm volatile("" : "+v"(xk[i]));
      SCHED();
    }
#pragma unroll
    for (int j = 0; j < 8; ++j) xk[j] = trd<ROR8>(xk[j], xk[j + 8], (lane & 8) != 0);
#pragma unroll
    for (int j = 0; j < 4; ++j) xk[j] = trd<HMIR>(xk[j], xk[j + 4], (lane & 4) != 0);
#pragma unroll
    for (int j = 0; j < 2; ++j) xk[j] = trd<XOR2>(xk[j], xk[j + 2], (lane & 2) != 0);
    const float dot = trd<XOR1>(xk[0], xk[1], (lane & 1) != 0);
    sc = rbf(rbf(dot) * 0.08838834764831845f);
  }
  const float mw = max64(sc);
  const float p = rbf(ex2((sc - mw) * LOG2E));
  const unsigned pp = (__float_as_uint(p) >> 16) | (__float_as_uint(p) & 0xffff0000u);
  { const float lw = sum64(p); if (lane == 0) { lds[L_WM + w] = mw; lds[L_WL + w] = lw; } }
  STAMP(6);
  v4f acc = {0.f, 0.f, 0.f, 0.f};
  v4u dm;
  { const unsigned hm = c16 < 8 ? ((c16 & 1) ? 0xffff0000u : 0x0000ffffu) : 0u;
    dm.x = (c16 >> 1) == 0 ? hm : 0u; dm.y = (c16 >> 1) == 1 ? hm : 0u; dm.z = (c16 >> 1) == 2 ? hm : 0u; dm.w = (c16 >> 1) == 3 ? hm : 0u; }
#define PV_ACC(i, reg) { const unsigned pb = dppu<BCAST + (i)>(pp); const v4u bm = {pb & dm.x, pb & dm.y, pb & dm.z, pb & dm.w}; acc = mfma16(reg, bm, acc); }
#define PIN_ACC() asm volatile("" : "+v"(acc))
#define PV_LANDED(i) if ((i) < VP) PV_ACC(i, vp[(i) < VP ? (i) : 0]) \
    else if ((i) < VP + VL) { const v4u t_ = *(LAS v4u*)&lds[L_VT + (w * VL + ((i) - VP)) * 256 + lane * 4]; PV_ACC(i, t_) }
  constexpr int S0 = 16 + VP + VL;
  constexpr int NL = VR + 2;
#define LATE(n) if ((n) < NL) { paceq(S0 + ((n) < NL - 1 - BURST ? (n) : NL - 1 - BURST)); \
    if ((n) == 1) wot[0] = ld_w(ro, voff_o, 0); else if ((n) == 3) wot[1] = ld_w(ro, voff_o, 4 * 1280); \
    else { constexpr int vi = (n) == 0 ? 0 : (n) == 2 ? 1 : (n) - 2; kv[vi < VR ? vi : 0] = ld_v(rv, lane * 16, toff(VP + VL + vi)); } SCHED(); }
  LATE(0)
  VM_WAIT(1);
  PV_LANDED(0) PV_LANDED(1) PV_LANDED(2) PV_LANDED(3) PV_LANDED(4) PV_LANDED(5)
  PV_LANDED(6) PV_LANDED(7) PV_LANDED(8) PV_LANDED(9) PV_LANDED(10) PV_LANDED(11)
  PIN_ACC(); SCHED();
  LATE(1) LATE(2) LATE(3) LATE(4) LATE(5) LATE(6) LATE(7) LATE(8) LATE(9) LATE(10) LATE(11) LATE(12) LATE(13)
  STAMP(44);
#undef PV_LANDED
#undef LATE
#define PV_LATE(i) if ((i) >= VP + VL) PV_ACC(i, kv[(i) >= VP + VL ? (i) - VP - VL : 0])
  PV_LATE(0) PV_LATE(1) PV_LATE(2) PV_LATE(3) PV_LATE(4) PV_LATE(5) PV_LATE(6) PV_LATE(7)
  PV_LATE(8) PV_LATE(9) PV_LATE(10) PV_LATE(11) PV_LATE(12) PV_LATE(13) PV_LATE(14) PV_LATE(15)
#undef PV_LATE
#undef PV_ACC
  PIN_ACC();
  STAMP(7);
  if (c16 < 8) *(LAS v4f*)&lds[L_PART + w * 132 + c16 * 16 + 4 * rho] = acc;
  gut[0] = ld_w(rg, (lane >> 3) * 3456 + (lane & 7) * 16, 0); gut[1] = ld_w(ru, (lane >> 3) * 3456 + (lane & 7) * 16, 0);
  dnt = ld_w(rd, (lane >> 3) * 1280 + (lane & 7) * 16, 0);
  SCHED();
  BAR();
  STAMP(8);
  float bmul = 0.f;
  v4u em;
  { const unsigned hm = (lane & 1) ? 0xffff0000u : 0x0000ffffu; const int m = (lane & 7) >> 1;
    em.x = m == 0 ? hm : 0u; em.y = m == 1 ? hm : 0u; em.z = m == 2 ? hm : 0u; em.w = m == 3 ? hm : 0u; }
  v4f o4;
  if (DM) {
    const float wm = lds[L_WM + c16], wl = lds[L_WL + c16];
    const v2f pv = *(LAS v2f*)&lds[L_PART + c16 * 132 + 8 * w + 2 * rho];
    const float Mcu = max_row16(wm);
    const float fk = ex2((wm - Mcu) * LOG2E);
    const float a0 = sum_row16(fk * pv.x), a1 = sum_row16(fk * pv.y);
    auto r0 = __builtin_amdgcn_permlane16_swap(__float_as_uint(a0), __float_as_uint(a0), false, false);
    auto r1 = __builtin_amdgcn_permlane16_swap(__float_as_uint(a1), __float_as_uint(a1), false, false);
    const bool ml = w == 15 && lane == 1;
    const float Lcu = sum_row16(fk * wl);
    const int vo = (lane & 31) == 0 ? (2 * w + hi) * 16 : ml ? 512 : 0x7fffff00;
    st_p(rws, vo, WS_O + (h * 8 + s) * 640, ml ? Mcu : a0, ml ? Lcu : a1, __uint_as_float(r0[1]), __uint_as_float(r1[1]), tg);
  }
  if (w == 15) {
    __builtin_amdgcn_s_setprio(3);
    if (!DM) {
    const float wm_a = lds[L_WM + c16], wm_b = lds[L_WM + (c16 & 7) + 8 * hi], wl_a = lds[L_WL + c16];
    v4f pv[8];
#pragma unroll
    for (int k = 0; k < 8; ++k) pv[k] = *(LAS v4f*)&lds[L_PART + (k + 8 * hi) * 132 + 4 * (lane & 31)];
    SCHED();
    const float Mcu = max_row16(wm_a);
    const float fk = ex2((wm_b - Mcu) * LOG2E);
    const float Lcu = sum_row16(ex2((wm_a - Mcu) * LOG2E) * wl_a);
    o4 = v4f{0.f, 0.f, 0.f, 0.f};
#define MRG(k) { const float f = dpp<BCAST + (k)>(fk); o4 += f * pv[k]; }
    MRG(0) MRG(1) MRG(2) MRG(3) MRG(4) MRG(5) MRG(6) MRG(7)
#undef MRG
    {
      const float a = tr32(o4.x, o4.x), bq = tr32(o4.y, o4.y), c = tr32(o4.z, o4.z), d = tr32(o4.w, o4.w);
      const bool ml = lane == 32;
      st_p(rws, lane <= 32 ? lane * 16 : 0x7fffff00, WS_O + (h * 8 + s) * 640, ml ? Mcu : a, ml ? Lcu : bq, c, d, tg);
    }
    }
    STAMP(9);
    if (D1) __builtin_amdgcn_s_sleep(D1);
    not_before(X1);
    v4u go[5];
    const unsigned np1 = pollp<5, 2>(rws, [&](int k) { return k < 4 ? (2 * k + hi) * 640 + (lane & 31) * 16 : (lane & 7) * 640 + 512; },
                                     [&](int) { return WS_O + h * 8 * 640; }, true, tg, go);
    STAMPV(29, np1);
    STAMP(10);
    const float m = upk(go[4].x), l = upk(go[4].y);
    const float M = max_lo8(m);
    const float f = ex2((m - M) * LOG2E);
    const float rL = rcp(sum_lo8(f * l));
    o4 = v4f{0.f, 0.f, 0.f, 0.f};
#define CMB(u) { const float f0 = __uint_as_float(__builtin_amdgcn_readlane(__float_as_uint(f), 2 * (u))), f1 = __uint_as_float(__builtin_amdgcn_readlane(__float_as_uint(f), 2 * (u) + 1)); \
      const float fb = hi ? f1 : f0; o4.x = fmaf(fb, upk(go[u].x), o4.x); o4.y = fmaf(fb, upk(go[u].y), o4.y); o4.z = fmaf(fb, upk(go[u].z), o4.z); o4.w = fmaf(fb, upk(go[u].w), o4.w); }
    CMB(0) CMB(1) CMB(2) CMB(3)
#undef CMB
    { const int pos = 4 * (lane & 31) + 2 * hi, dim = 8 * (pos & 15) + (pos >> 4);
      lds[L_OS + dim] = rbf(tr32(o4.x, o4.z) * rL); lds[L_OS + dim + 8] = rbf(tr32(o4.y, o4.w) * rL); }
  } else if (gw) {
    bmul = rbf(1.f + lds[L_MOD + 4 * 80 + 8 * w + r8]);
    const v4u ab = vand(bfpair(bmul), em), ac = vand(bfpair(lds[L_MOD + 3 * 80 + 8 * w + r8]), em);
    const v4f z4 = {0.f, 0.f, 0.f, 0.f};
    const v4f tb_ = fold2(mfma16(ab, gut[0], z4), mfma16(ab, gut[1], z4)), tc_ = fold2(mfma16(ac, gut[0], z4), mfma16(ac, gut[1], z4));
    if (c16 < 8) { *(LAS v4f*)&lds[L_PB + w * 128 + hi * 64 + 8 * c16 + 4 * (rho & 1)] = tb_; *(LAS v4f*)&lds[L_PC + w * 128 + hi * 64 + 8 * c16 + 4 * (rho & 1)] = tc_; }
  }
  STAMP(45);
  BAR();
  STAMP(11);
  {
    const float o0 = lds[L_OS + 8 * w + rho], o1 = lds[L_OS + 8 * w + 4 + rho];
    const v4f z4 = {0.f, 0.f, 0.f, 0.f};
    const v4f y = mfma16(vand(bfpair(o1), dm), wot[1], mfma16(vand(bfpair(o0), dm), wot[0], z4));
    if (rho < 2 && c16 < 10) *(LAS v4f*)&lds[L_PY + w * 84 + 8 * c16 + 4 * rho] = y;
  }
  STAMP(12);
  BAR();
  STAMP(13);
  if (DY) {
  if (gw) {
    const v2f pv = *(LAS v2f*)&lds[L_PY + c16 * 84 + 8 * w + 2 * rho];
    const float a0 = sum_row16(pv.x), a1 = sum_row16(pv.y);
    auto r0 = __builtin_amdgcn_permlane16_swap(__float_as_uint(a0), __float_as_uint(a0), false, false);
    auto r1 = __builtin_amdgcn_permlane16_swap(__float_as_uint(a1), __float_as_uint(a1), false, false);
    st_p(rws, (lane & 31) == 0 ? hi * 16 : 0x7fffff00, WS_Y + ((s * 10 + w) * 32 + h) * 32, a0, a1, __uint_as_float(r0[1]), __uint_as_float(r1[1]), tg);
    STAMP(14);
  }
  } else {
  if (w == 15) {
    v4f pv[16];
    const int j20 = lane < 20 ? lane : 0;
#pragma unroll
    for (int k = 0; k < 16; ++k) pv[k] = *(LAS v4f*)&lds[L_PY + k * 84 + 4 * j20];
    SCHED();
    v4f ya = (pv[0] + pv[1]) + (pv[2] + pv[3]), yb = (pv[4] + pv[5]) + (pv[6] + pv[7]), yc = (pv[8] + pv[9]) + (pv[10] + pv[11]), yd = (pv[12] + pv[13]) + (pv[14] + pv[15]);
    ya = (ya + yb) + (yc + yd);
    st_p(rws, lane < 20 ? (lane >> 1) * 1024 + (lane & 1) * 16 : 0x7fffff00, WS_Y + (s * 10 * 32 + h) * 32, ya.x, ya.y, ya.z, ya.w, tg);
    STAMP(14);
  }
  }
  const int g = ffn ? h : 0, r = s;
  const int aoff = ((8 * g + r8) * 8 + r) * 128 + (lane & 7) * 16;
  if (w == 10) {
    float pb[10], pu[10], cb[10], cu[10];
#pragma unroll
    for (int k = 0; k < 10; ++k) { pb[k] = lds[L_PB + k * 128 + lane]; pu[k] = lds[L_PB + k * 128 + 64 + lane]; cb[k] = lds[L_PC + k * 128 + lane]; cu[k] = lds[L_PC + k * 128 + 64 + lane]; }
    SCHED();
#define SUM10(p) ((((p)[0] + (p)[1]) + ((p)[2] + (p)[3])) + (((p)[4] + (p)[5]) + ((p)[6] + (p)[7])) + ((p)[8] + (p)[9]))
    st_p(rws, ffn ? aoff : 0x7fffff00, WS_BC, SUM10(pb), SUM10(pu), SUM10(cb), SUM10(cu), tg);
  }
  const bool fin = b >= 236 && w == 14;
  const int fk2 = b - 236;
  if (fin) {
    v4u gg[1];
    poll<1, 1>(rws, (rho * 1920 + (lane & 15)) * 16, [&](int) { return WS_MOD + (5 * 320 + 16 * fk2) * 16; }, true, tA, tB, gg);
    const float g0 = tr32(tr16(__uint_as_float(gg[0].x), __uint_as_float(gg[0].x)), tr16(__uint_as_float(gg[0].x), __uint_as_float(gg[0].x)));
    const float g1 = tr32(tr16(__uint_as_float(gg[0].z), __uint_as_float(gg[0].z)), tr16(__uint_as_float(gg[0].z), __uint_as_float(gg[0].z)));
    if (lane < 16) *(LAS v2f*)&lds[L_GB + 2 * lane] = v2f{rbf(g0), rbf(g1)};
  }
  const int coff = ((9 * drho + (w < 9 ? w : 0)) * 8 + (lane & 7)) * 128 + r8 * 16;
  v4u gbc;
  if (gw) {
    const float xres = lds[L_XS + 8 * w + r8], gam = lds[L_MOD + 2 * 80 + 8 * w + r8];
    asm volatile("" :: "v"(xres), "v"(gam));
    if (D2) __builtin_amdgcn_s_sleep(D2);
    not_before(X2);
    v4u gy[1];
    STAMP(47);
    const unsigned np2 = pollp<1, PSL>(rws, [&](int) { return (lane & 31) * 32 + hi * 16; }, [&](int) { return WS_Y + (r * 10 + w) * 1024; }, true, tg, gy);
    STAMPV(26, np2);
    STAMP(16);
    gbc = __builtin_amdgcn_raw_buffer_load_b128(rws, coff, WS_BC, AUX_SC1);
    const float ya = tr16(upk(gy[0].x), upk(gy[0].z)), yb = tr16(upk(gy[0].y), upk(gy[0].w));
    const float attn = rbf(sum_lo8(trd<ROR8>(ya, yb, (lane & 8) != 0)));
    const int cl = 8 * w + r8;
    const float x1 = rbf(xres + rbf(gam * attn));
    const float xs = x1 * bmul, xh = rbf(xs), xl = rbf(xs - xh);
    const v4u ah = vand(bfpair(xh), em), al = vand(bfpair(xl), em);
    const v4f z4 = {0.f, 0.f, 0.f, 0.f};
    const v4f ta = fold2(mfma16(al, gut[0], mfma16(ah, gut[0], z4)), mfma16(al, gut[1], mfma16(ah, gut[1], z4)));
    if (c16 < 8) *(LAS v4f*)&lds[L_PA + w * 128 + hi * 64 + 8 * c16 + 4 * (rho & 1)] = ta;
    const float mean_w = sum_hi8(x1) * 0.125f;
    const float dd = x1 - mean_w;
    const float m2_w = sum_hi8(dd * dd);
    if (lane == 0) *(LAS v2f*)&lds[L_STAT + 2 * w] = v2f{mean_w, m2_w};
    if ((lane & 7) == 0) lds[L_X1 + cl] = x1;
  }
  STAMP(17);
  BAR();
  STAMP(18);
  if (w == 9) {
    float pa[10], pb_[10];
#pragma unroll
    for (int k = 0; k < 10; ++k) { pa[k] = lds[L_PA + k * 128 + lane]; pb_[k] = lds[L_PA + k * 128 + 64 + lane]; }
    const v2f sv = *(LAS v2f*)&lds[L_STAT + 2 * (c16 < 10 ? c16 : 0)];
    SCHED();
    const float mean_r = sum_row16(c16 < 10 ? sv.x : 0.f) * 0.1f;
    const float dd = sv.x - mean_r;
    const float m2 = sum_row16(c16 < 10 ? fmaf(8.f * dd, dd, sv.y) : 0.f);
    st_p(rws, ffn ? aoff : 0x7fffff00, WS_A, SUM10(pa), SUM10(pb_), mean_r, m2, tg);
    STAMP(19);
  } else if (w == 10) {
    if (h == 0 && lane < 20) { const v4f xv = *(LAS v4f*)&lds[L_X1 + 4 * lane]; st_p(rws, lane * 16, WS_X1 + r * 320, xv.x, xv.y, xv.z, xv.w, tg); }
  }
  if (w < 9) {
    if (!__all(bad_p(gbc, tg) == 0u || b >= 240)) { v4u t_[1]; pollp<1, 2>(rws, [&](int) { return coff; }, [&](int) { return WS_BC; }, b < 240, tg, t_); gbc = t_[0]; }
    const float Bg = sum_lo8(upk(gbc.x)), Bu = sum_lo8(upk(gbc.y)), Cg = sum_lo8(upk(gbc.z)), Cu = sum_lo8(upk(gbc.w));
    if (D3) __builtin_amdgcn_s_sleep(D3);
    not_before(X3);
    STAMP(25);
    v4u ga[1];
    const unsigned np3 = pollp<1, 1>(rws, [&](int) { return coff; }, [&](int) { return WS_A; }, b < 240, tg, ga);
    STAMPV(27, np3);
    STAMP(20);
    const float Ag = sum_lo8(upk(ga[0].x)), Au = sum_lo8(upk(ga[0].y));
    const float mr = upk(ga[0].z);
    const float mean = sum_lo8(mr) * 0.125f;
    const float dd = mr - mean;
    const float var = sum_lo8(fmaf(80.f * dd, dd, upk(ga[0].w))) * (1.f / D);
    const float mb = rbf(mean), den = rbf(__builtin_amdgcn_sqrtf(rbf(rbf(var) + 1e-5f)));
    const float rden = rcp(den);
    const float gate = rbf(fmaf(Ag - mb * Bg, rden, Cg)), up = rbf(fmaf(Au - mb * Bu, rden, Cu));
    const float act = rbf(rbf(silu_fast(gate)) * up);
    const v4f z4 = {0.f, 0.f, 0.f, 0.f};
    const v4f dd4 = mfma16(vand(bfpair(act), em), dnt, z4);
    const v4f td = fold2(dd4, dd4);
    if (lane < 32 && c16 < 8) *(LAS v4f*)&lds[L_PD + w * 64 + 8 * c16 + 4 * rho] = td;
  }
  STAMP(21);
  BAR();
  if (w == 15) {
    v4f pv[9];
#pragma unroll
    for (int k = 0; k < 9; ++k) pv[k] = *(LAS v4f*)&lds[L_PD + k * 64 + 4 * c16];
    SCHED();
    const v4f d = (((pv[0] + pv[1]) + (pv[2] + pv[3])) + ((pv[4] + pv[5]) + (pv[6] + pv[7]))) + pv[8];
    st_p(rws, (lane < 16 && b < 240) ? (lane >> 3) * (24 * 128) + (lane & 7) * 16 : 0x7fffff00, WS_D + (dkap * 2 * 24 + drho) * 128, d.x, d.y, d.z, d.w, tg);
    STAMP(22);
  }
  if (fin) {
    v4u gx[1];
    pollp<1, 2>(rws, [&](int) { return (lane & 7) * 16; }, [&](int) { return WS_X1 + fk2 * 128; }, true, tg, gx);
    const v4f gbv = *(LAS v4f*)&lds[L_GB + 4 * (lane & 7)];
    if (D4) __builtin_amdgcn_s_sleep(D4);
    not_before(X4);
    STAMP(23);
    v4u gd[3];
    const unsigned np4 = pollp<3, 1>(rws, [&](int) { return lane * 16; }, [&](int k) { return WS_D + fk2 * 24 * 128 + k * 1024; }, true, tg, gd);
    STAMPV(28, np4);
    STAMP(24);
    const float e0 = sum_hi8((upk(gd[0].x) + upk(gd[1].x)) + upk(gd[2].x)), e1 = sum_hi8((upk(gd[0].y) + upk(gd[1].y)) + upk(gd[2].y));
    const float e2 = sum_hi8((upk(gd[0].z) + upk(gd[1].z)) + upk(gd[2].z)), e3 = sum_hi8((upk(gd[0].w) + upk(gd[1].w)) + upk(gd[2].w));
    if (lane < 8) {
      const float o0 = upk(gx[0].x) + rbf(gbv.x * rbf(e0)), o1 = upk(gx[0].y) + rbf(gbv.y * rbf(e1));
      const float o2 = upk(gx[0].z) + rbf(gbv.z * rbf(e2)), o3 = upk(gx[0].w) + rbf(gbv.w * rbf(e3));
      uint2 ov; ov.x = bfbits(o0) | (bfbits(o1) << 16); ov.y = bfbits(o2) | (bfbits(o3) << 16);
      *(uint2*)(out + 32 * fk2 + 4 * lane) = ov;
      if (b == 236 && lane == 0) *(uint2*)(ws + WS_EPOCH) = make_uint2(tA, tB);
    }
  }
  STAMP(30);
  STAMPV(49, *tb_lds);
  STAMPV(48, (unsigned)__builtin_amdgcn_s_getreg(20 | (3 << 11)) & 0xfu);
  if (CPF) asm volatile("adaln_end_%c0:" :: "i"(UID));
  { const u64 dc = __builtin_amdgcn_s_memtime() - clk0; if (lane == 31) tsv = dc; }
  ((u64*)(ws + WS_DBG))[(size_t)(b * 16 + w) * 64 + lane] = tsv;
}

template <int KWIN, int VP, int VL, int PSL, int D1, int D2, int D3, int D4, int TOUCH, int DT, int T0S, int TM, int DQ, int CPF, int CTB, int KW2, int X1, int X2, int X3, int X4, int TLBT, int BURST, int DM, int DY>
static void launch(void* const* d_in, void* d_out, void* d_ws, hipStream_t stream) {
  adaln_decode_kernel<KWIN, VP, VL, PSL, D1, D2, D3, D4, TOUCH, DT, T0S, TM, DQ, CPF, CTB, KW2, X1, X2, X3, X4, TLBT, BURST, DM, DY><<<256, 1024, 0, stream>>>(
      (const u16*)d_in[0], (const u16*)d_in[1], (const u16*)d_in[2], (const u16*)d_in[3], (const u16*)d_in[4], (const u16*)d_in[6],
      (const u16*)d_in[7], (const u16*)d_in[8], (const u16*)d_in[9], (const u16*)d_in[10], (u16*)d_out, (unsigned char*)d_ws);
}

__global__ void k_wait(const unsigned char* ws) {
  __shared__ unsigned long long mn, mx;
  const u64* hs = (const u64*)(ws + WS_DBG);
  if (threadIdx.x == 0) { mn = ~0ull; mx = 0; }
  __syncthreads();
  for (int i = threadIdx.x; i < 4096; i += blockDim.x) atomicMin(&mn, hs[i * 64]);
  __syncthreads();
  for (int i = threadIdx.x; i < 4096; i += blockDim.x) { u64 v = hs[i * 64 + 0]; if (v > mn && v - mn < 100000) atomicMax(&mx, v - mn); }
  __syncthreads();
  if (threadIdx.x == 0) { const u64 t0 = rt(); const u64 d = mx * 30; while (rt() - t0 < d) __builtin_amdgcn_s_sleep(8); }
}

}

extern "C" void kernel_launch(void* const* d_in, const int* in_sizes, int n_in, void* d_out, int out_size, void* d_ws, size_t ws_size,
                              hipStream_t stream) {
  adaln::launch<4, 6, 4, 2, 8, 16, 12, 8, 2, 1040, 60, 350, 16, 1, 1, 6, 0, 0, 0, 0, 1, 0, 0, 1>(d_in, d_out, d_ws, stream);
  adaln::k_wait<<<1, 1024, 0, stream>>>((const unsigned char*)d_ws);
}
```

```cpp
#include <hip/hip_runtime.h>
#include <stdint.h>

namespace adaln {

typedef unsigned short u16;
typedef unsigned long long u64;
typedef unsigned v4u __attribute__((ext_vector_type(4)));
typedef float v2f __attribute__((ext_vector_type(2)));
typedef float v4f __attribute__((ext_vector_type(4)));
typedef __bf16 bf16x2 __attribute__((ext_vector_type(2)));
typedef __bf16 bf16x8 __attribute__((ext_vector_type(8)));
#define LAS __attribute__((address_space(3)))

constexpr int D = 640, SK = 8192;
constexpr int AUX_NT = 2, AUX_SC1 = 16;
constexpr int RSRC3 = 0x00020000;
constexpr float LOG2E = 1.4426950408889634f;
constexpr unsigned SPIN_CAP = 1u << 16;

constexpr int KWIN = 4, KW2 = 6;
constexpr int VP = 6, VL = 4, VR = 16 - VP - VL;
constexpr int T0S = 150, DT = 970;
constexpr int PRIO = 30;
constexpr int TM = 350;
constexpr int D1 = 10, D2 = 4, D3 = 20, D4 = 16;

constexpr int WS_EPOCH = 0;
constexpr int WS_T0 = 128;
constexpr int WS_MOD = 256;
constexpr int WS_Q = WS_MOD + 4 * 1920 * 16;
constexpr int WS_O = WS_Q + 32 * 8 * 64 * 16;
constexpr int WS_Y = WS_O + 32 * 8 * 640;
constexpr int WS_A = WS_Y + 8 * 10 * 32 * 2 * 16;
constexpr int WS_BC = WS_A + 216 * 8 * 128;
constexpr int WS_X1 = WS_BC + 216 * 8 * 128;
constexpr int WS_D = WS_X1 + 160 * 16;
constexpr int WS_END = WS_D + 20 * 24 * 128;

constexpr int L_Q = 0;
constexpr int L_FLAG = L_Q + 64;
constexpr int L_HS = L_FLAG + 4;
constexpr int L_MOD = L_HS + 80;
constexpr int L_WM = L_MOD + 480;
constexpr int L_WL = L_WM + 16;
constexpr int L_STAT = L_WL + 16;
constexpr int L_X1 = L_STAT + 32;
constexpr int L_XS = L_X1 + 80;
constexpr int L_OS = L_XS + 80;
constexpr int L_GB = L_OS + 128;
constexpr int L_PD = L_GB + 32;
constexpr int L_PART = L_PD + 576;
constexpr int L_PY = L_PART + 2112;
constexpr int L_PA = L_PY + 1344;
constexpr int L_PB = L_PA + 1280;
constexpr int L_PC = L_PB + 1280;
constexpr int L_VT = L_PC + 1280;
static_assert(L_VT % 4 == 0 && L_PART % 4 == 0 && L_VT - L_PART >= 5120, "alignment; room for the wq tile");

__device__ __forceinline__ float bflo(unsigned u) { return __uint_as_float(u << 16); }
__device__ __forceinline__ float bfhi(unsigned u) { return __uint_as_float(u & 0xffff0000u); }
__device__ __forceinline__ float rbf(float f) { float r = (float)(__bf16)f; asm("" : "+v"(r)); return r; }
__device__ __forceinline__ unsigned bfbits(float f) { return (unsigned)__builtin_bit_cast(u16, (__bf16)f); }
__device__ __forceinline__ float ex2(float x) { return __builtin_amdgcn_exp2f(x); }
__device__ __forceinline__ float silu(float x) { return x / (1.f + ex2(-x * LOG2E)); }
__device__ __forceinline__ float rcp(float x) { return __builtin_amdgcn_rcpf(x); }
__device__ __forceinline__ float silu_fast(float x) { return x * rcp(1.f + ex2(-x * LOG2E)); }
__device__ __forceinline__ float dot2(unsigned a, unsigned b, float acc) {
  return __builtin_amdgcn_fdot2_f32_bf16(__builtin_bit_cast(bf16x2, a), __builtin_bit_cast(bf16x2, b), acc, false);
}
__device__ __forceinline__ v4f mfma16(const v4u& a, const v4u& b, const v4f& c) {
  return __builtin_amdgcn_mfma_f32_16x16x32_bf16(__builtin_bit_cast(bf16x8, a), __builtin_bit_cast(bf16x8, b), c, 0, 0, 0);
}
__device__ __forceinline__ unsigned bfpair(float exact_bf16) { const unsigned u = __float_as_uint(exact_bf16); return (u >> 16) | (u & 0xffff0000u); }
__device__ __forceinline__ v4u vand(unsigned a, const v4u& m) { return v4u{a & m.x, a & m.y, a & m.z, a & m.w}; }
template <int CTRL> __device__ __forceinline__ unsigned dppu(unsigned x) { return (unsigned)__builtin_amdgcn_mov_dpp((int)x, CTRL, 0xf, 0xf, true); }
template <int CTRL> __device__ __forceinline__ float dpp(float x) {
  return __builtin_bit_cast(float, __builtin_amdgcn_mov_dpp(__builtin_bit_cast(int, x), CTRL, 0xf, 0xf, true));
}
constexpr int XOR1 = 0xB1, XOR2 = 0x4E, HMIR = 0x141, MIR = 0x140, ROR8 = 0x128, BCAST = 0x150;
__device__ __forceinline__ float tr32(float a, float b) {
  auto r = __builtin_amdgcn_permlane32_swap(__float_as_uint(a), __float_as_uint(b), false, false);
  return __uint_as_float(r[0]) + __uint_as_float(r[1]);
}
__device__ __forceinline__ float tr16(float a, float b) {
  auto r = __builtin_amdgcn_permlane16_swap(__float_as_uint(a), __float_as_uint(b), false, false);
  return __uint_as_float(r[0]) + __uint_as_float(r[1]);
}
__device__ __forceinline__ float max32(float a) {
  auto r = __builtin_amdgcn_permlane32_swap(__float_as_uint(a), __float_as_uint(a), false, false);
  return fmaxf(__uint_as_float(r[0]), __uint_as_float(r[1]));
}
__device__ __forceinline__ float max16(float a) {
  auto r = __builtin_amdgcn_permlane16_swap(__float_as_uint(a), __float_as_uint(a), false, false);
  return fmaxf(__uint_as_float(r[0]), __uint_as_float(r[1]));
}
template <int CTRL> __device__ __forceinline__ float trd(float a, float b, bool hi) {
  const float t1 = a + dpp<CTRL>(a), t2 = b + dpp<CTRL>(b);
  return hi ? t2 : t1;
}
__device__ __forceinline__ float sum_row16(float x) { x += dpp<XOR1>(x); x += dpp<XOR2>(x); x += dpp<HMIR>(x); x += dpp<MIR>(x); return x; }
__device__ __forceinline__ float max_row16(float x) { x = fmaxf(x, dpp<XOR1>(x)); x = fmaxf(x, dpp<XOR2>(x)); x = fmaxf(x, dpp<HMIR>(x)); return fmaxf(x, dpp<MIR>(x)); }
__device__ __forceinline__ float sum_lo8(float x) { x += dpp<XOR1>(x); x += dpp<XOR2>(x); x += dpp<HMIR>(x); return x; }
__device__ __forceinline__ float max_lo8(float x) { x = fmaxf(x, dpp<XOR1>(x)); x = fmaxf(x, dpp<XOR2>(x)); x = fmaxf(x, dpp<HMIR>(x)); return x; }
__device__ __forceinline__ float sum_hi8(float x) { x += dpp<ROR8>(x); x = tr16(x, x); x = tr32(x, x); return x; }
__device__ __forceinline__ float sum64(float x) { x = sum_row16(x); x = tr16(x, x); return tr32(x, x); }
__device__ __forceinline__ float max64(float x) { return max32(max16(max_row16(x))); }
__device__ __forceinline__ v4f fold2(const v4f& a, const v4f& b) {
  v4f t;
#pragma unroll
  for (int i = 0; i < 4; ++i) {
    auto r = __builtin_amdgcn_permlane32_swap(__float_as_uint(a[i]), __float_as_uint(b[i]), false, false);
    t[i] = __uint_as_float(r[0]) + dpp<0x128>(__uint_as_float(r[1]));
  }
  return t;
}
__device__ __forceinline__ void unpack8(const v4u& w, float (&f)[8]) {
  f[0] = bflo(w.x); f[1] = bfhi(w.x); f[2] = bflo(w.y); f[3] = bfhi(w.y); f[4] = bflo(w.z); f[5] = bfhi(w.z); f[6] = bflo(w.w); f[7] = bfhi(w.w);
}

typedef __amdgpu_buffer_rsrc_t rsrc_t;
__device__ __forceinline__ rsrc_t mk(const void* p, unsigned bytes) { return __builtin_amdgcn_make_buffer_rsrc((void*)p, 0, (int)bytes, RSRC3); }
__device__ __forceinline__ v4u ld_nt(rsrc_t r, int voff, int soff) { return __builtin_amdgcn_raw_buffer_load_b128(r, voff, soff, AUX_NT); }
__device__ __forceinline__ v4u ld_g(rsrc_t r, int voff, int soff) { return __builtin_amdgcn_raw_buffer_load_b128(r, voff, soff, AUX_SC1); }
__device__ __forceinline__ void st_g(rsrc_t r, int voff, int soff, float a, float b, unsigned tA, unsigned tB) {
  v4u v = {__float_as_uint(a), tA, __float_as_uint(b), tB};
  __builtin_amdgcn_raw_buffer_store_b128(v, r, voff, soff, AUX_SC1);
}
struct Tag4 { unsigned a, b, c, d; };
__device__ __forceinline__ unsigned pk(float v, unsigned t) { return ((__float_as_uint(v) + 0x800u) & 0xfffff000u) | t; }
__device__ __forceinline__ float upk(unsigned u) { return __uint_as_float(u & 0xfffff000u); }
__device__ __forceinline__ void st_p(rsrc_t r, int voff, int soff, float a, float b, float c, float d, const Tag4& t) {
  v4u v = {pk(a, t.a), pk(b, t.b), pk(c, t.c), pk(d, t.d)};
  __builtin_amdgcn_raw_buffer_store_b128(v, r, voff, soff, AUX_SC1);
}
__device__ __forceinline__ unsigned bad_p(const v4u& g, const Tag4& t) { return ((g.x ^ t.a) | (g.y ^ t.b) | (g.z ^ t.c) | (g.w ^ t.d)) & 0xfffu; }
template <int N, int SLP, typename VOFF, typename SOFF>
__device__ __forceinline__ unsigned pollp(rsrc_t rs, VOFF voff, SOFF soff, bool active, const Tag4& t, v4u (&g)[N]) {
  unsigned spins = 0;
  for (; spins < SPIN_CAP; ++spins) {
    asm volatile("" ::: "memory");
#pragma unroll
    for (int k = 0; k < N; ++k) g[k] = __builtin_amdgcn_raw_buffer_load_b128(rs, voff(k), soff(k), AUX_SC1);
    unsigned bad = 0;
#pragma unroll
    for (int k = 0; k < N; ++k) bad |= bad_p(g[k], t);
    if (__all(bad == 0u || !active)) break;
    if (SLP) __builtin_amdgcn_s_sleep(SLP);
  }
  return spins;
}
__device__ __forceinline__ bool tag_ok(const v4u& g, unsigned tA, unsigned tB) { return g.y == tA && g.w == tB; }
__device__ __forceinline__ void glds(const void* gsrc, unsigned lds_dst) {
  unsigned keep;
  asm volatile("s_mov_b32 %0, m0\n\ts_mov_b32 m0, %2\n\ts_nop 0\n\tglobal_load_lds_dwordx4 %1, off nt\n\ts_mov_b32 m0, %0"
               : "=&s"(keep) : "v"(gsrc), "s"(lds_dst) : "memory");
}
#define LDS_WAIT() asm volatile("s_waitcnt lgkmcnt(0)" ::: "memory")
#define VM_WAIT(n) asm volatile("s_waitcnt vmcnt(%0)" :: "i"(n) : "memory")
#define BAR() do { asm volatile("s_waitcnt lgkmcnt(0)" ::: "memory"); __builtin_amdgcn_s_barrier(); asm volatile("" ::: "memory"); } while (0)
#define SCHED() __builtin_amdgcn_sched_barrier(0)

template <int N, int SLP, typename OFF>
__device__ __forceinline__ unsigned poll(rsrc_t rs, int voff, OFF soff, bool active, unsigned tA, unsigned tB, v4u (&g)[N]) {
  unsigned spins = 0;
  for (; spins < SPIN_CAP; ++spins) {
    asm volatile("" ::: "memory");
    bool ok = true;
#pragma unroll
    for (int k = 0; k < N; ++k) g[k] = ld_g(rs, voff, soff(k));
#pragma unroll
    for (int k = 0; k < N; ++k) ok = ok && tag_ok(g[k], tA, tB);
    if (__all(ok || !active)) break;
    if (SLP) __builtin_amdgcn_s_sleep(SLP);
  }
  return spins;
}
__device__ __forceinline__ u64 rt() { return __builtin_amdgcn_s_memrealtime(); }

__global__ void __launch_bounds__(1024, 4)
adaln_decode_kernel(const u16* __restrict__ x, const u16* __restrict__ Kc, const u16* __restrict__ Vc, const u16* __restrict__ wq,
                    const u16* __restrict__ wo, const u16* __restrict__ wg, const u16* __restrict__ wu, const u16* __restrict__ wd,
                    const u16* __restrict__ cnd, const u16* __restrict__ wmod, u16* __restrict__ out, unsigned char* __restrict__ ws) {
  __shared__ __attribute__((aligned(16))) float lds[L_VT + 16 * VL * 256];
  const int tid = threadIdx.x, lane = tid & 63, w = __builtin_amdgcn_readfirstlane(tid >> 6);
  const int b = blockIdx.x;
  const int h = (b & 7) * 4 + (b >> 6), s = (b >> 3) & 7;
  const int rho = lane >> 4, c16 = lane & 15;
  const int fg = b >> 3, fr = b & 7;
  const int drho = b < 240 ? (b & 7) + 8 * ((b >> 3) / 10) : 0, dkap = (b >> 3) % 10;
  asm volatile("" :: "s"(x), "s"(Kc), "s"(Vc), "s"(wq), "s"(cnd), "s"(wmod), "s"(ws));
  volatile LAS unsigned* qflag = (volatile LAS unsigned*)&lds[L_FLAG];
  if (tid == 0) *qflag = 0u;
  const rsrc_t rws = mk(ws, WS_END);
  const size_t kvbase = ((size_t)h * SK + s * 1024) * 256;
  const rsrc_t rk = mk((const char*)Kc + kvbase, 262144);
  const rsrc_t rv = mk((const char*)Vc + kvbase, 262144);
  const int rot = ((b >> 3) * 5 + (b & 7) * 3) & 15;
  auto toff = [&](int i) { return ((((i + rot) & 15) << 4) + w) << 10; };
  typedef unsigned v2u __attribute__((ext_vector_type(2)));
  const v2u ep = __builtin_amdgcn_raw_buffer_load_b64(rws, 0, WS_EPOCH, 0);
  unsigned tA, tB; Tag4 tg;
  auto get_tags = [&]() {
    tA = __builtin_amdgcn_readfirstlane(ep.x) + 1u; tB = __builtin_amdgcn_readfirstlane(ep.y) + 1u;
    u64 z = (((u64)tB << 32) | tA) * 0x9E3779B97F4A7C15ull; z ^= z >> 29; z *= 0xBF58476D1CE4E5B9ull; z ^= z >> 32;
    tg.a = (unsigned)z & 0xfffu; tg.b = (unsigned)(z >> 12) & 0xfffu; tg.c = (unsigned)(z >> 24) & 0xfffu; tg.d = (unsigned)(z >> 36) & 0xfffu;
  };

  const unsigned tbase = (unsigned)rt();
  volatile LAS unsigned* tb_lds = (volatile LAS unsigned*)&lds[L_FLAG + 1];
  if (tid == 0) *tb_lds = tbase;
  auto pace_w = [&](int n) {
    const unsigned due = (unsigned)(T0S + n * DT / 16), tb = *tb_lds;
    int late = (int)((unsigned)rt() - tb - due);
    if (late > 2 * PRIO) __builtin_amdgcn_s_setprio(3); else if (late > PRIO) __builtin_amdgcn_s_setprio(2); else if (late > 0) __builtin_amdgcn_s_setprio(1); else __builtin_amdgcn_s_setprio(0);
    while (late < 0) { __builtin_amdgcn_s_sleep(1); late = (int)((unsigned)rt() - tb - due); }
  };
  auto pace = [&](int n) { VM_WAIT(KWIN - 1); pace_w(n); };
  auto paceq = [&](int n) { VM_WAIT(KW2 - 1); pace_w(n); };
  v4u kv[16], vp[VP];
  const unsigned vt_lds = (unsigned)(uintptr_t)(LAS float*)&lds[L_VT] + (unsigned)w * (VL * 1024);
  const char* vgl = (const char*)Vc + kvbase + lane * 16;
  const int cb = b % 60, rq = b / 60;
  const bool has_mod = b < 240;
  const int r8 = lane >> 3, hi = lane >> 5;
  unsigned pf0 = 0, pf1 = 0, pf2 = 0, pf3 = 0;
  v4u mt[2]; u16 cv[2];
  unsigned xw[5], xs2 = 0, xr2 = 0;
  if (w < 10) {
    const int r0 = (has_mod ? rq : 0) * 160 + 16 * w;
    const rsrc_t rm = mk((const char*)wmod + (size_t)r0 * 7680 + (has_mod ? cb : 0) * 128, has_mod ? 16 * 7680 : 0);
    mt[0] = ld_nt(rm, r8 * 7680 + (lane & 7) * 16, 0); mt[1] = ld_nt(rm, r8 * 7680 + (lane & 7) * 16, 8 * 7680);
    cv[0] = cnd[r0 + r8]; cv[1] = cnd[r0 + 8 + r8];
    if (w == 0) {
#pragma unroll
      for (int k = 0; k < 5; ++k) xw[k] = ((const unsigned*)x)[lane + 64 * k];
      xs2 = ((const unsigned*)x)[40 * s + (lane < 40 ? lane : 0)];
      xr2 = ((const unsigned*)x)[40 * fr + (lane < 40 ? lane : 0)];
    }
  } else if (w < 15) {
    const char* gq = (const char*)wq + (size_t)(s * 80 + 16 * (w - 10) + rho) * 8192 + h * 256 + c16 * 16;
    const unsigned dst = (unsigned)(uintptr_t)(LAS float*)&lds[L_PART] + (w - 10) * 4096;
#pragma unroll
    for (int u = 0; u < 4; ++u) glds(gq + (size_t)u * 4 * 8192, dst + u * 1024);
  } else {
    {
      u64 pc; unsigned len;
      asm volatile("s_getpc_b64 %0\n.Lpc%=:\n\ts_mov_b32 %1, adaln_kernel_end-.Lpc%=" : "=s"(pc), "=s"(len));
      const unsigned off = (unsigned)(b >> 3) * 1024 + (lane & 7) * 64;
      const rsrc_t rc = mk((const void*)pc, len);
      pf0 = __builtin_amdgcn_raw_buffer_load_b32(rc, off, 0, 0); pf1 = __builtin_amdgcn_raw_buffer_load_b32(rc, off ^ 512, 0, 0);
    }
    {
      const char* pt = lane == 0 ? (const char*)Kc + kvbase : lane == 1 ? (const char*)Vc + kvbase : lane == 2 ? (const char*)wo + (size_t)(h * 128) * 1280
                     : lane == 3 ? (const char*)wg + (size_t)(80 * fr) * 3456 : lane == 4 ? (const char*)wu + (size_t)(80 * fr) * 3456
                     : (lane == 5 || b < 236) ? (const char*)wd + (size_t)(72 * drho) * 1280 : (const char*)(out + 32 * (b - 236));
      pf3 = __builtin_nontemporal_load((const unsigned*)pt);
    }
    {
      const int own = WS_O + (h * 8 + s) * 640;
      int o0;
      if (lane < 5) o0 = own + lane * 128;
      else if (lane < 15) o0 = WS_Y + ((s * 10 + (lane - 5)) * 32 + h) * 32;
      else if (lane < 23) o0 = fg < 27 ? WS_A + ((8 * fg + (lane - 15)) * 8 + fr) * 128 : own;
      else if (lane < 31) o0 = fg < 27 ? WS_BC + ((8 * fg + (lane - 23)) * 8 + fr) * 128 : own;
      else if (lane < 33) o0 = b < 240 ? WS_D + ((dkap * 2 + (lane - 31)) * 24 + drho) * 128 : own;
      else o0 = fg == 0 ? WS_X1 + fr * 320 : own;
      pf2 = __builtin_amdgcn_raw_buffer_load_b32(rws, o0, 0, AUX_SC1);
    }
  }
  SCHED();
  if (w != 0) { while ((unsigned)rt() - tbase < (unsigned)T0S) __builtin_amdgcn_s_sleep(1); kv[0] = ld_nt(rk, lane * 16, toff(0)); SCHED(); }
  if (w < 10) {
    const float s0 = rbf(silu(bflo(cv[0]))), s1 = rbf(silu(bflo(cv[1])));
    float f0[8], f1[8]; unpack8(mt[0], f0); unpack8(mt[1], f1);
    float y[4];
#pragma unroll
    for (int j = 0; j < 4; ++j) y[j] = tr32(fmaf(s0, f0[j], s1 * f1[j]), fmaf(s0, f0[j + 4], s1 * f1[j + 4]));
    float z0 = tr16(y[0], y[2]), z1 = tr16(y[1], y[3]);
    z0 += dpp<ROR8>(z0); z1 += dpp<ROR8>(z1);
    if ((lane & 8) == 0) *(LAS v2f*)&lds[L_PC + w * 64 + 8 * (lane & 7) + 2 * rho] = v2f{z0, z1};
  } else if (w < 15) {
    VM_WAIT(1);
  }
  BAR();
  SCHED();
  get_tags();
  if (w == 0) {
    {
      v2f pv[10];
#pragma unroll
      for (int k = 0; k < 10; ++k) pv[k] = *(LAS v2f*)&lds[L_PC + k * 64 + 2 * (lane & 31)];
      const v2f v = (((pv[0] + pv[1]) + (pv[2] + pv[3])) + ((pv[4] + pv[5]) + (pv[6] + pv[7]))) + (pv[8] + pv[9]);
      asm volatile("" :: "v"(v.x), "v"(v.y));
      if (has_mod && lane < 32) st_g(rws, lane * 16, WS_MOD + (rq * 1920 + cb * 32) * 16, v.x, v.y, tA, tB);
      if (b == 0 && lane == 0) st_g(rws, 0, WS_T0, __uint_as_float(tbase), 0.f, tA, tB);
    }
    float mean_b, den_b;
    {
      float xv[10];
#pragma unroll
      for (int k = 0; k < 5; ++k) { xv[2 * k] = bflo(xw[k]); xv[2 * k + 1] = bfhi(xw[k]); }
      float sm = 0.f;
#pragma unroll
      for (int k = 0; k < 10; ++k) sm += xv[k];
      const float mean = sum64(sm) * (1.f / D);
      float vs = 0.f;
#pragma unroll
      for (int k = 0; k < 10; ++k) { const float d = xv[k] - mean; vs = fmaf(d, d, vs); }
      const float var = sum64(vs) * (1.f / D);
      mean_b = rbf(mean); den_b = rbf(sqrtf(rbf(rbf(var) + 1e-5f)));
    }
    const int j40 = lane < 40 ? lane : 0;
    if (lane < 40) *(LAS v2f*)&lds[L_XS + 2 * lane] = v2f{bflo(xr2), bfhi(xr2)};
    {
      v4u g[8];
      while ((unsigned)rt() - tbase < (unsigned)TM) __builtin_amdgcn_s_sleep(2);
      poll<8, 2>(rws, j40 * 16, [&](int k) { return WS_MOD + ((k & 3) * 1920 + (k >> 2) * 320 + 40 * s) * 16; }, lane < 40, tA, tB, g);
      const float sa0 = rbf((__uint_as_float(g[0].x) + __uint_as_float(g[1].x)) + (__uint_as_float(g[2].x) + __uint_as_float(g[3].x)));
      const float sa1 = rbf((__uint_as_float(g[0].z) + __uint_as_float(g[1].z)) + (__uint_as_float(g[2].z) + __uint_as_float(g[3].z)));
      const float ca0 = rbf((__uint_as_float(g[4].x) + __uint_as_float(g[5].x)) + (__uint_as_float(g[6].x) + __uint_as_float(g[7].x)));
      const float ca1 = rbf((__uint_as_float(g[4].z) + __uint_as_float(g[5].z)) + (__uint_as_float(g[6].z) + __uint_as_float(g[7].z)));
      const float h0 = rbf(rbf(rbf(rbf(bflo(xs2) - mean_b) / den_b) * rbf(1.f + ca0)) + sa0);
      const float h1 = rbf(rbf(rbf(rbf(bfhi(xs2) - mean_b) / den_b) * rbf(1.f + ca1)) + sa1);
      if (lane < 40) *(LAS v2f*)&lds[L_HS + 2 * lane] = v2f{h0, h1};
      VM_WAIT(0);
      LDS_WAIT();
    }
    {
      float acc[8];
#pragma unroll
      for (int j = 0; j < 8; ++j) acc[j] = 0.f;
#pragma unroll
      for (int u = 0; u < 20; ++u) {
        const float hv = lds[L_HS + 4 * u + rho];
        const v4u tq = *(LAS v4u*)&lds[L_PART + u * 256 + lane * 4];
        float f[8]; unpack8(tq, f);
#pragma unroll
        for (int j = 0; j < 8; ++j) acc[j] = fmaf(hv, f[j], acc[j]);
      }
      float y[4];
#pragma unroll
      for (int j = 0; j < 4; ++j) y[j] = tr32(acc[j], acc[j + 4]);
      const float z0 = tr16(y[0], y[2]), z1 = tr16(y[1], y[3]);
      st_g(rws, lane * 16, WS_Q + (h * 8 + s) * 1024, z0, z1, tA, tB);
    }
    {
      v4u g[8];
      {
        const v4u gt = ld_g(rws, 0, WS_T0);
        if (tag_ok(gt, tA, tB) && (int)(gt.x - tbase) < 0 && (int)(tbase - gt.x) < 400 && lane == 0) *tb_lds = gt.x;
      }
      poll<8, 4>(rws, lane * 16, [&](int k) { return WS_Q + (h * 8 + k) * 1024; }, true, tA, tB, g);
      float q0 = 0.f, q1 = 0.f;
#pragma unroll
      for (int k = 0; k < 8; ++k) { q0 += __uint_as_float(g[k].x); q1 += __uint_as_float(g[k].z); }
      ((LAS unsigned*)lds)[L_Q + 4 * c16 + rho] = bfbits(q0) | (bfbits(q1) << 16);
    }
    LDS_WAIT();
    *qflag = 1u;
#pragma unroll
    for (int i = 0; i < 16; ++i) kv[i] = ld_nt(rk, lane * 16, toff(i));
    SCHED();
#pragma unroll
    for (int i = 0; i < VP; ++i) vp[i] = ld_nt(rv, lane * 16, toff(i));
#pragma unroll
    for (int i = 0; i < VL; ++i) glds(vgl + toff(VP + i), vt_lds + i * 1024);
    SCHED();
    VM_WAIT(0);
  } else {
#pragma unroll
    for (int i = 1; i < 16; ++i) { pace(i); kv[i] = ld_nt(rk, lane * 16, toff(i)); SCHED(); }
#pragma unroll
    for (int i = 0; i < VP; ++i) { pace(16 + i); vp[i] = ld_nt(rv, lane * 16, toff(i)); SCHED(); }
#pragma unroll
    for (int i = 0; i < VL; ++i) { pace(16 + VP + i); glds(vgl + toff(VP + i), vt_lds + i * 1024); SCHED(); }
    asm volatile("" :: "v"(pf0), "v"(pf1), "v"(pf2), "v"(pf3));
    for (unsigned spins = 0; spins < (SPIN_CAP << 6); ++spins) { if (*qflag != 0u) break; __builtin_amdgcn_s_sleep(1); }
    asm volatile("" ::: "memory");
  }
  const bool ffn = fg < 27, gw = w < 10;
  const rsrc_t ro = mk((const char*)wo + (size_t)(h * 128 + 8 * w) * 1280 + s * 160, 8 * 1280);
  const int voff_o = c16 < 10 ? rho * 1280 + c16 * 16 : 0x7fffff00;
  const bool dw = b < 240 && w < 9;
  v4u wot[2], gut[2], dnt;
  const v4u qf = *(LAS v4u*)&lds[L_Q + 4 * c16];
  float sc;
  {
    float xk[16];
#pragma unroll
    for (int i = 0; i < 16; ++i) {
      float d = dot2(kv[i].x, qf.x, 0.f); d = dot2(kv[i].y, qf.y, d); d = dot2(kv[i].z, qf.z, d); d = dot2(kv[i].w, qf.w, d);
      xk[i] = d;
      asm volatile("" : "+v"(xk[i]));
      SCHED();
    }
#pragma unroll
    for (int j = 0; j < 8; ++j) xk[j] = trd<ROR8>(xk[j], xk[j + 8], (lane & 8) != 0);
#pragma unroll
    for (int j = 0; j < 4; ++j) xk[j] = trd<HMIR>(xk[j], xk[j + 4], (lane & 4) != 0);
#pragma unroll
    for (int j = 0; j < 2; ++j) xk[j] = trd<XOR2>(xk[j], xk[j + 2], (lane & 2) != 0);
    const float dot = trd<XOR1>(xk[0], xk[1], (lane & 1) != 0);
    sc = rbf(rbf(dot) * 0.08838834764831845f);
  }
  const float mw = max64(sc);
  const float p = rbf(ex2((sc - mw) * LOG2E));
  const unsigned pp = (__float_as_uint(p) >> 16) | (__float_as_uint(p) & 0xffff0000u);
  { const float lw = sum64(p); if (lane == 0) { lds[L_WM + w] = mw; lds[L_WL + w] = lw; } }
  v4f acc = {0.f, 0.f, 0.f, 0.f};
  v4u dm;
  { const unsigned hm = c16 < 8 ? ((c16 & 1) ? 0xffff0000u : 0x0000ffffu) : 0u;
    dm.x = (c16 >> 1) == 0 ? hm : 0u; dm.y = (c16 >> 1) == 1 ? hm : 0u; dm.z = (c16 >> 1) == 2 ? hm : 0u; dm.w = (c16 >> 1) == 3 ? hm : 0u; }
#define PV_ACC(i, reg) { const unsigned pb = dppu<BCAST + (i)>(pp); const v4u bm = {pb & dm.x, pb & dm.y, pb & dm.z, pb & dm.w}; acc = mfma16(reg, bm, acc); }
#define PIN_ACC() asm volatile("" : "+v"(acc))
#define PV_LANDED(i) if ((i) < VP) PV_ACC(i, vp[(i) < VP ? (i) : 0]) \
    else if ((i) < VP + VL) { const v4u t_ = *(LAS v4u*)&lds[L_VT + (w * VL + ((i) - VP)) * 256 + lane * 4]; PV_ACC(i, t_) }
  constexpr int S0 = 16 + VP + VL;
  constexpr int NL = VR + 2;
#define LATE(n) if ((n) < NL) { paceq(S0 + (n)); \
    if ((n) == 1) wot[0] = ld_nt(ro, voff_o, 0); else if ((n) == 3) wot[1] = ld_nt(ro, voff_o, 4 * 1280); \
    else { constexpr int vi = (n) - ((n) > 1 ? 1 : 0) - ((n) > 3 ? 1 : 0); kv[vi < VR ? vi : 0] = ld_nt(rv, lane * 16, toff(VP + VL + vi)); } SCHED(); }
  LATE(0)
  VM_WAIT(1);
  PV_LANDED(0) PV_LANDED(1) PV_LANDED(2) PV_LANDED(3) PV_LANDED(4) PV_LANDED(5)
  PV_LANDED(6) PV_LANDED(7) PV_LANDED(8) PV_LANDED(9) PV_LANDED(10) PV_LANDED(11)
  PIN_ACC(); SCHED();
  v4u gm[4];
  const int j40 = lane < 40 ? lane : 0;
  const bool modw = w >= 1 && w <= 4;
  if (modw) {
#pragma unroll
    for (int k = 0; k < 4; ++k) gm[k] = ld_g(rws, j40 * 16, WS_MOD + (k * 1920 + (w + 1) * 320 + 40 * fr) * 16);
  }
  SCHED();
  LATE(1) LATE(2)
  if (modw) {
    if (!__all((tag_ok(gm[0], tA, tB) && tag_ok(gm[1], tA, tB) && tag_ok(gm[2], tA, tB) && tag_ok(gm[3], tA, tB)) || lane >= 40))
      poll<4, 1>(rws, j40 * 16, [&](int k) { return WS_MOD + (k * 1920 + (w + 1) * 320 + 40 * fr) * 16; }, lane < 40, tA, tB, gm);
    const float m0 = rbf((__uint_as_float(gm[0].x) + __uint_as_float(gm[1].x)) + (__uint_as_float(gm[2].x) + __uint_as_float(gm[3].x)));
    const float m1 = rbf((__uint_as_float(gm[0].z) + __uint_as_float(gm[1].z)) + (__uint_as_float(gm[2].z) + __uint_as_float(gm[3].z)));
    if (lane < 40) *(LAS v2f*)&lds[L_MOD + (w + 1) * 80 + 2 * lane] = v2f{m0, m1};
  }
  SCHED();
  LATE(3)
  const int r8_ = lane >> 3, hi_ = lane >> 5;
  const int g = ffn ? fg : 0, r = fr;
  const bool fin = b >= 236 && w == 14;
  const int fk2 = b - 236;
  u64 pg_ = (u64)wg + (size_t)(80 * r + 8 * (gw ? w : 0)) * 3456 + g * 128, pu_ = (u64)wu + (size_t)(80 * r + 8 * (gw ? w : 0)) * 3456 + g * 128;
  u64 pd_ = (u64)wd + (size_t)(72 * drho + 8 * (w < 9 ? w : 0)) * 1280 + dkap * 128;
  int szg = (ffn && gw) ? 8 * 3456 : 0, szd = dw ? 8 * 1280 : 0;
  int so_o = WS_O + (h * 8 + s) * 640, so_oh = WS_O + h * 8 * 640, so_yp = WS_Y + ((s * 10 + w) * 32 + h) * 32, so_yc = WS_Y + (r * 10 + w) * 1024;
  int so_d = WS_D + (dkap * 2 * 24 + drho) * 128;
  int vo_g = r8_ * 3456 + (lane & 7) * 16, vo_d = r8_ * 1280 + (lane & 7) * 16;
  int aoff = ffn ? ((8 * g + r8_) * 8 + r) * 128 + (lane & 7) * 16 : 0x7fffff00;
  int coff = ((9 * drho + (w < 9 ? w : 0)) * 8 + (lane & 7)) * 128 + r8_ * 16;
  int vo_o = lane <= 32 ? lane * 16 : 0x7fffff00, vo_oa = hi_ * 640 + (lane & 31) * 16, vo_ob = (lane & 7) * 640 + 512;
  int vo_yp = (lane & 31) == 0 ? hi_ * 16 : 0x7fffff00, vo_yc = (lane & 31) * 32 + hi_ * 16;
  v4u em;
  { const unsigned hm = (lane & 1) ? 0xffff0000u : 0x0000ffffu; const int m = (lane & 7) >> 1;
    em.x = m == 0 ? hm : 0u; em.y = m == 1 ? hm : 0u; em.z = m == 2 ? hm : 0u; em.w = m == 3 ? hm : 0u; }
  asm volatile("" : "+s"(pg_), "+s"(pu_), "+s"(pd_), "+s"(szg), "+s"(szd), "+s"(tg.a), "+s"(tg.b), "+s"(tg.c), "+s"(tg.d));
  asm volatile("" : "+s"(so_o), "+s"(so_oh), "+s"(so_yp), "+s"(so_yc), "+s"(so_d));
  asm volatile("" : "+v"(vo_g), "+v"(vo_d), "+v"(aoff), "+v"(coff), "+v"(vo_o), "+v"(vo_oa), "+v"(vo_ob), "+v"(vo_yp), "+v"(vo_yc), "+v"(em));
  SCHED();
  LATE(4) LATE(5) LATE(6) LATE(7) LATE(8) LATE(9) LATE(10) LATE(11) LATE(12) LATE(13)
#undef PV_LANDED
#undef LATE
#define PV_LATE(i) if ((i) >= VP + VL) PV_ACC(i, kv[(i) >= VP + VL ? (i) - VP - VL : 0])
  PV_LATE(0) PV_LATE(1) PV_LATE(2) PV_LATE(3) PV_LATE(4) PV_LATE(5) PV_LATE(6) PV_LATE(7)
  PV_LATE(8) PV_LATE(9) PV_LATE(10) PV_LATE(11) PV_LATE(12) PV_LATE(13) PV_LATE(14) PV_LATE(15)
#undef PV_LATE
#undef PV_ACC
  PIN_ACC();
  __builtin_amdgcn_s_setprio(0);
  if (c16 < 8) *(LAS v4f*)&lds[L_PART + w * 132 + c16 * 16 + 4 * rho] = acc;
  BAR();
  float bmul = 0.f;
  v4f o4;
  if (w == 15) {
    __builtin_amdgcn_s_setprio(3);
    const float wm_a = lds[L_WM + c16], wm_b = lds[L_WM + (c16 & 7) + 8 * hi], wl_a = lds[L_WL + c16];
    v4f pv[8];
#pragma unroll
    for (int k = 0; k < 8; ++k) pv[k] = *(LAS v4f*)&lds[L_PART + (k + 8 * hi) * 132 + 4 * (lane & 31)];
    SCHED();
    const float Mcu = max_row16(wm_a);
    const float fk = ex2((wm_b - Mcu) * LOG2E);
    const float Lcu = sum_row16(ex2((wm_a - Mcu) * LOG2E) * wl_a);
    o4 = v4f{0.f, 0.f, 0.f, 0.f};
#define MRG(k) { const float f = dpp<BCAST + (k)>(fk); o4 += f * pv[k]; }
    MRG(0) MRG(1) MRG(2) MRG(3) MRG(4) MRG(5) MRG(6) MRG(7)
#undef MRG
    {
      const float a = tr32(o4.x, o4.x), bq = tr32(o4.y, o4.y), c = tr32(o4.z, o4.z), d = tr32(o4.w, o4.w);
      const bool ml = lane == 32;
      st_p(rws, vo_o, so_o, ml ? Mcu : a, ml ? Lcu : bq, c, d, tg);
    }
    __builtin_amdgcn_s_sleep(D1);
    v4u go[5];
    pollp<5, 2>(rws, [&](int k) { return k < 4 ? vo_oa : vo_ob; },
                                     [&](int k) { return so_oh + (k < 4 ? k * 1280 : 0); }, true, tg, go);
    const float m = upk(go[4].x), l = upk(go[4].y);
    const float M = max_lo8(m);
    const float f = ex2((m - M) * LOG2E);
    const float rL = rcp(sum_lo8(f * l));
    o4 = v4f{0.f, 0.f, 0.f, 0.f};
#define CMB(u) { const float f0 = __uint_as_float(__builtin_amdgcn_readlane(__float_as_uint(f), 2 * (u))), f1 = __uint_as_float(__builtin_amdgcn_readlane(__float_as_uint(f), 2 * (u) + 1)); \
      const float fb = hi ? f1 : f0; o4.x = fmaf(fb, upk(go[u].x), o4.x); o4.y = fmaf(fb, upk(go[u].y), o4.y); o4.z = fmaf(fb, upk(go[u].z), o4.z); o4.w = fmaf(fb, upk(go[u].w), o4.w); }
    CMB(0) CMB(1) CMB(2) CMB(3)
#undef CMB
    { const int pos = 4 * (lane & 31) + 2 * hi, dim = 8 * (pos & 15) + (pos >> 4);
      lds[L_OS + dim] = rbf(tr32(o4.x, o4.z) * rL); lds[L_OS + dim + 8] = rbf(tr32(o4.y, o4.w) * rL); }
  }
  BAR();
  if (gw) { gut[0] = ld_nt(mk((const void*)pg_, szg), vo_g, 0); gut[1] = ld_nt(mk((const void*)pu_, szg), vo_g, 0); dnt = ld_nt(mk((const void*)pd_, szd), vo_d, 0); }
  SCHED();
  {
    const float o0 = lds[L_OS + 8 * w + rho], o1 = lds[L_OS + 8 * w + 4 + rho];
    const v4f z4 = {0.f, 0.f, 0.f, 0.f};
    const v4f y = mfma16(vand(bfpair(o1), dm), wot[1], mfma16(vand(bfpair(o0), dm), wot[0], z4));
    if (rho < 2 && c16 < 10) *(LAS v4f*)&lds[L_PY + w * 84 + 8 * c16 + 4 * rho] = y;
  }
  BAR();
  if (gw) {
    const v2f pv = *(LAS v2f*)&lds[L_PY + c16 * 84 + 8 * w + 2 * rho];
    const float a0 = sum_row16(pv.x), a1 = sum_row16(pv.y);
    auto r0 = __builtin_amdgcn_permlane16_swap(__float_as_uint(a0), __float_as_uint(a0), false, false);
    auto r1 = __builtin_amdgcn_permlane16_swap(__float_as_uint(a1), __float_as_uint(a1), false, false);
    st_p(rws, vo_yp, so_yp, a0, a1, __uint_as_float(r0[1]), __uint_as_float(r1[1]), tg);
  }
#define SUM10(p) ((((p)[0] + (p)[1]) + ((p)[2] + (p)[3])) + (((p)[4] + (p)[5]) + ((p)[6] + (p)[7])) + ((p)[8] + (p)[9]))
  if (gw) {
    bmul = rbf(1.f + lds[L_MOD + 4 * 80 + 8 * w + r8]);
    const v4u ab = vand(bfpair(bmul), em), ac = vand(bfpair(lds[L_MOD + 3 * 80 + 8 * w + r8]), em);
    const v4f z4 = {0.f, 0.f, 0.f, 0.f};
    const v4f tb_ = fold2(mfma16(ab, gut[0], z4), mfma16(ab, gut[1], z4)), tc_ = fold2(mfma16(ac, gut[0], z4), mfma16(ac, gut[1], z4));
    if (c16 < 8) { *(LAS v4f*)&lds[L_PB + w * 128 + hi * 64 + 8 * c16 + 4 * (rho & 1)] = tb_; *(LAS v4f*)&lds[L_PC + w * 128 + hi * 64 + 8 * c16 + 4 * (rho & 1)] = tc_; }
  }
  if (fin) {
    v4u gg[1];
    poll<1, 1>(rws, (rho * 1920 + (lane & 15)) * 16, [&](int) { return WS_MOD + (5 * 320 + 16 * fk2) * 16; }, true, tA, tB, gg);
    const float g0 = tr32(tr16(__uint_as_float(gg[0].x), __uint_as_float(gg[0].x)), tr16(__uint_as_float(gg[0].x), __uint_as_float(gg[0].x)));
    const float g1 = tr32(tr16(__uint_as_float(gg[0].z), __uint_as_float(gg[0].z)), tr16(__uint_as_float(gg[0].z), __uint_as_float(gg[0].z)));
    if (lane < 16) *(LAS v2f*)&lds[L_GB + 2 * lane] = v2f{rbf(g0), rbf(g1)};
  }
  if (gw) {
    const float xres = lds[L_XS + 8 * w + r8], gam = lds[L_MOD + 2 * 80 + 8 * w + r8];
    asm volatile("" :: "v"(xres), "v"(gam));
    __builtin_amdgcn_s_sleep(D2);
    v4u gy[1];
    pollp<1, 2>(rws, [&](int) { return vo_yc; }, [&](int) { return so_yc; }, true, tg, gy);
    const float ya = tr16(upk(gy[0].x), upk(gy[0].z)), yb = tr16(upk(gy[0].y), upk(gy[0].w));
    const float attn = rbf(sum_lo8(trd<ROR8>(ya, yb, (lane & 8) != 0)));
    const int cl = 8 * w + r8;
    const float x1 = rbf(xres + rbf(gam * attn));
    const float xs = x1 * bmul, xh = rbf(xs), xl = rbf(xs - xh);
    const v4u ah = vand(bfpair(xh), em), al = vand(bfpair(xl), em);
    const v4f z4 = {0.f, 0.f, 0.f, 0.f};
    const v4f ta = fold2(mfma16(al, gut[0], mfma16(ah, gut[0], z4)), mfma16(al, gut[1], mfma16(ah, gut[1], z4)));
    if (c16 < 8) *(LAS v4f*)&lds[L_PA + w * 128 + hi * 64 + 8 * c16 + 4 * (rho & 1)] = ta;
    const float mean_w = sum_hi8(x1) * 0.125f;
    const float dd = x1 - mean_w;
    const float m2_w = sum_hi8(dd * dd);
    if (lane == 0) *(LAS v2f*)&lds[L_STAT + 2 * w] = v2f{mean_w, m2_w};
    if ((lane & 7) == 0) lds[L_X1 + cl] = x1;
  }
  BAR();
  if (w == 9) {
    float pa[10], pb_[10];
#pragma unroll
    for (int k = 0; k < 10; ++k) { pa[k] = lds[L_PA + k * 128 + lane]; pb_[k] = lds[L_PA + k * 128 + 64 + lane]; }
    const v2f sv = *(LAS v2f*)&lds[L_STAT + 2 * (c16 < 10 ? c16 : 0)];
    SCHED();
    const float mean_r = sum_row16(c16 < 10 ? sv.x : 0.f) * 0.1f;
    const float dd = sv.x - mean_r;
    const float m2 = sum_row16(c16 < 10 ? fmaf(8.f * dd, dd, sv.y) : 0.f);
    st_p(rws, aoff, WS_A, SUM10(pa), SUM10(pb_), mean_r, m2, tg);
  } else if (w == 10) {
    float pb[10], pu[10], cb[10], cu[10];
#pragma unroll
    for (int k = 0; k < 10; ++k) { pb[k] = lds[L_PB + k * 128 + lane]; pu[k] = lds[L_PB + k * 128 + 64 + lane]; cb[k] = lds[L_PC + k * 128 + lane]; cu[k] = lds[L_PC + k * 128 + 64 + lane]; }
    SCHED();
    st_p(rws, aoff, WS_BC, SUM10(pb), SUM10(pu), SUM10(cb), SUM10(cu), tg);
  } else if (w == 11) {
    if (fg == 0 && lane < 20) { const v4f xv = *(LAS v4f*)&lds[L_X1 + 4 * lane]; st_p(rws, lane * 16, WS_X1 + r * 320, xv.x, xv.y, xv.z, xv.w, tg); }
  }
  if (w < 9) {
    __builtin_amdgcn_s_sleep(D3);
    v4u ga[2];
    pollp<2, 1>(rws, [&](int) { return coff; }, [&](int k) { return k == 0 ? WS_A : WS_BC; }, b < 240, tg, ga);
    const float Bg = sum_lo8(upk(ga[1].x)), Bu = sum_lo8(upk(ga[1].y)), Cg = sum_lo8(upk(ga[1].z)), Cu = sum_lo8(upk(ga[1].w));
    const float Ag = sum_lo8(upk(ga[0].x)), Au = sum_lo8(upk(ga[0].y));
    const float mr = upk(ga[0].z);
    const float mean = sum_lo8(mr) * 0.125f;
    const float dd = mr - mean;
    const float var = sum_lo8(fmaf(80.f * dd, dd, upk(ga[0].w))) * (1.f / D);
    const float mb = rbf(mean), den = rbf(__builtin_amdgcn_sqrtf(rbf(rbf(var) + 1e-5f)));
    const float rden = rcp(den);
    const float gate = rbf(fmaf(Ag - mb * Bg, rden, Cg)), up = rbf(fmaf(Au - mb * Bu, rden, Cu));
    const float act = rbf(rbf(silu_fast(gate)) * up);
    const v4f z4 = {0.f, 0.f, 0.f, 0.f};
    const v4f dd4 = mfma16(vand(bfpair(act), em), dnt, z4);
    const v4f td = fold2(dd4, dd4);
    if (lane < 32 && c16 < 8) *(LAS v4f*)&lds[L_PD + w * 64 + 8 * c16 + 4 * rho] = td;
  }
  v4u gx[1];
  if (fin) {
    __builtin_amdgcn_s_sleep(36);
    pollp<1, 2>(rws, [&](int) { return (lane & 7) * 16; }, [&](int) { return WS_X1 + fk2 * 128; }, true, tg, gx);
  }
  BAR();
  if (w == 15) {
    v4f pv[9];
#pragma unroll
    for (int k = 0; k < 9; ++k) pv[k] = *(LAS v4f*)&lds[L_PD + k * 64 + 4 * c16];
    SCHED();
    const v4f d = (((pv[0] + pv[1]) + (pv[2] + pv[3])) + ((pv[4] + pv[5]) + (pv[6] + pv[7]))) + pv[8];
    st_p(rws, (lane < 16 && b < 240) ? (lane >> 3) * (24 * 128) + (lane & 7) * 16 : 0x7fffff00, so_d, d.x, d.y, d.z, d.w, tg);
  }
  if (fin) {
    const v4f gbv = *(LAS v4f*)&lds[L_GB + 4 * (lane & 7)];
    __builtin_amdgcn_s_sleep(D4);
    v4u gd[3];
    pollp<3, 1>(rws, [&](int) { return lane * 16; }, [&](int k) { return WS_D + fk2 * 24 * 128 + k * 1024; }, true, tg, gd);
    const float e0 = sum_hi8((upk(gd[0].x) + upk(gd[1].x)) + upk(gd[2].x)), e1 = sum_hi8((upk(gd[0].y) + upk(gd[1].y)) + upk(gd[2].y));
    const float e2 = sum_hi8((upk(gd[0].z) + upk(gd[1].z)) + upk(gd[2].z)), e3 = sum_hi8((upk(gd[0].w) + upk(gd[1].w)) + upk(gd[2].w));
    if (lane < 8) {
      const float o0 = upk(gx[0].x) + rbf(gbv.x * rbf(e0)), o1 = upk(gx[0].y) + rbf(gbv.y * rbf(e1));
      const float o2 = upk(gx[0].z) + rbf(gbv.z * rbf(e2)), o3 = upk(gx[0].w) + rbf(gbv.w * rbf(e3));
      uint2 ov; ov.x = bfbits(o0) | (bfbits(o1) << 16); ov.y = bfbits(o2) | (bfbits(o3) << 16);
      *(uint2*)(out + 32 * fk2 + 4 * lane) = ov;
      if (b == 236 && lane == 0) *(uint2*)(ws + WS_EPOCH) = make_uint2(tA, tB);
    }
  }
  asm volatile("adaln_kernel_end:");
}

}

extern "C" void kernel_launch(void* const* d_in, const int* in_sizes, int n_in, void* d_out, int out_size, void* d_ws, size_t ws_size,
                              hipStream_t stream) {
  using adaln::u16;
  adaln::adaln_decode_kernel<<<256, 1024, 0, stream>>>(
      (const u16*)d_in[0], (const u16*)d_in[1], (const u16*)d_in[2], (const u16*)d_in[3], (const u16*)d_in[4], (const u16*)d_in[6],
      (const u16*)d_in[7], (const u16*)d_in[8], (const u16*)d_in[9], (const u16*)d_in[10], (u16*)d_out, (unsigned char*)d_ws);
}
```
